```python
import torch

def _softmax(x, axis=-1):
    return torch.softmax(x, dim=axis)
def _gelu(x):
    return torch.nn.functional.gelu(x, approximate="tanh")
def _silu(x):
    return torch.nn.functional.silu(x)
def _sigmoid(x):
    return torch.sigmoid(x)
def _layernorm(x, axis=-1, eps=1e-5):
    m = x.mean(dim=axis, keepdim=True)
    v = x.var(dim=axis, keepdim=True, unbiased=False)
    return (x - m) / torch.sqrt(v + eps)
def _scatter_add(tgt, idx, src, axis=0):
    return tgt.clone().index_add_(axis, idx, src)
def _segment_sum(src, seg, n):
    out = torch.zeros((n,) + src.shape[1:], dtype=src.dtype)
    return out.index_add_(0, seg, src)
def _pad(x, pw):
    flat = tuple(v for pair in reversed(pw) for v in pair)
    return torch.nn.functional.pad(x, flat)

kernel_name = 'standalone__global_local_windowed_global_tokens__prompt'

def setup_inputs():

    _w = (lambda a: (lambda t: t.to(torch.bfloat16) if t.is_floating_point() else t)(torch.as_tensor(a)))
    B, H, S, D = 1, 32, 2048, 128
    return {
        "q": _w((lambda *s: torch.randn(*s, dtype=torch.bfloat16) * 0.8)(B, H, S, D)),
        "k": _w((lambda *s: torch.randn(*s, dtype=torch.bfloat16) * 0.8)(B, H, S, D)),
        "v": _w((lambda *s: torch.randn(*s, dtype=torch.bfloat16) * 0.05)(B, H, S, D)),
        "window": 32,
        "n_global": 2,
    }

def reference(q, k, v, window, n_global):
    B, H, S, D = q.shape
    scale = D ** -0.5
    scores = torch.einsum("bhqd,bhkd->bhqk", q, k) * scale
    idx = torch.arange(S)
    dist = idx[:, None] - idx[None, :]
    local = (dist <= window) & (dist >= -window)
    is_global_q = (idx < n_global)[:, None]
    is_global_k = (idx < n_global)[None, :]
    mask = local | is_global_q | is_global_k
    scores = torch.where(mask[None, None], scores, -1e9)
    p = _softmax(scores, axis=-1)
    return torch.einsum("bhqk,bhkd->bhqd", p, v)
```

```cpp
#include <hip/hip_runtime.h>
#include <hip/hip_bf16.h>

typedef __attribute__((ext_vector_type(8))) __bf16 bf16x8;
typedef __attribute__((ext_vector_type(2))) __bf16 bf16x2;
typedef __attribute__((ext_vector_type(4))) float f32x4;
typedef __attribute__((ext_vector_type(2))) float f32x2;
typedef __attribute__((ext_vector_type(4))) unsigned u32x4;
typedef __attribute__((ext_vector_type(2))) unsigned u32x2;

constexpr int H = 32, S = 2048, D = 128;
constexpr int NTILE = S / 16;
#define NT 4
constexpr int WPB = 4;
constexpr int NWAVE_H = NTILE / NT;
constexpr int NBLK = H * NWAVE_H / WPB;
constexpr int BPH = NWAVE_H / WPB;
constexpr int PREC = 132;
constexpr float C2 = 0.08838834764831845f * 1.4426950408889634f;

__device__ __forceinline__ unsigned cvtpk(float lo, float hi) {
  f32x2 v = {lo, hi};
  bf16x2 r = __builtin_convertvector(v, bf16x2);
  return __builtin_bit_cast(unsigned, r);
}
__device__ __forceinline__ float xg_max(float x) {
  auto s = __builtin_amdgcn_permlane16_swap(__float_as_uint(x), __float_as_uint(x), false, false);
  x = fmaxf(__uint_as_float(s[0]), __uint_as_float(s[1]));
  auto t = __builtin_amdgcn_permlane32_swap(__float_as_uint(x), __float_as_uint(x), false, false);
  return fmaxf(__uint_as_float(t[0]), __uint_as_float(t[1]));
}
__device__ __forceinline__ float xg_sum(float x) {
  auto s = __builtin_amdgcn_permlane16_swap(__float_as_uint(x), __float_as_uint(x), false, false);
  x = __uint_as_float(s[0]) + __uint_as_float(s[1]);
  auto t = __builtin_amdgcn_permlane32_swap(__float_as_uint(x), __float_as_uint(x), false, false);
  return __uint_as_float(t[0]) + __uint_as_float(t[1]);
}
__device__ __forceinline__ f32x4 mfma(u32x4 a, u32x4 b, f32x4 c) {
  return __builtin_amdgcn_mfma_f32_16x16x32_bf16(__builtin_bit_cast(bf16x8, a), __builtin_bit_cast(bf16x8, b), c, 0, 0, 0);
}
struct Tile { u32x4 r[4]; };
__device__ __forceinline__ Tile ld_frag(const char* tile_base, unsigned off) {
  Tile t;
#pragma unroll
  for (int s = 0; s < 4; ++s) t.r[s] = *(const u32x4*)(tile_base + off + s * 64);
  return t;
}
__device__ __forceinline__ Tile ld_rows(const char* tile_base, unsigned off) {
  Tile t;
#pragma unroll
  for (int j = 0; j < 4; ++j) t.r[j] = *(const u32x4*)(tile_base + off + j * 256);
  return t;
}
struct VT { u32x2 f[8]; };
__device__ __forceinline__ VT transpose_v(const Tile& v) {
  VT o;
#pragma unroll
  for (int f = 0; f < 8; ++f) {
    const int w = f >> 1;
    const unsigned sel = (f & 1) ? 0x07060302u : 0x05040100u;
    o.f[f][0] = __builtin_amdgcn_perm(v.r[1][w], v.r[0][w], sel);
    o.f[f][1] = __builtin_amdgcn_perm(v.r[3][w], v.r[2][w], sel);
  }
  return o;
}
__device__ __forceinline__ f32x4 qk(const Tile& k, const Tile& q, f32x4 c) {
#pragma unroll
  for (int s = 0; s < 4; ++s) c = mfma(k.r[s], q.r[s], c);
  return c;
}

__global__ __launch_bounds__(64 * WPB, 1) void attn_main(const char* __restrict__ q, const char* __restrict__ k, const char* __restrict__ v,
                                                          const int* __restrict__ pw, const int* __restrict__ png, char* __restrict__ out, float* __restrict__ ws) {
  const int bid = blockIdx.x;
  const int xcd = bid & 7, idx = bid >> 3;
  const int head = xcd * (H / 8) + idx / BPH, chunk = idx % BPH;
  const int wave = __builtin_amdgcn_readfirstlane(threadIdx.x >> 6), lane = threadIdx.x & 63;
  const int c = lane & 15, g = lane >> 4;
  const int wh = chunk * WPB + wave;
  const int t0 = wh * NT;
  const size_t hb = (size_t)head * S * 256;
  const char* qh = q + hb; const char* kh = k + hb; const char* vh = v + hb; char* oh = out + hb;
  const unsigned offF = c * 256 + g * 16, offR = g * 1024 + c * 16;

  Tile Q[NT], K[NT + 4], V[NT + 4], Kg, Vg, Qg;
  auto clampt = [](int t) { return t < 0 ? 0 : (t > NTILE - 1 ? NTILE - 1 : t); };
  Q[0] = ld_frag(qh + t0 * 4096, offF);
#pragma unroll
  for (int u = 0; u < 5; ++u) K[u] = ld_frag(kh + clampt(t0 - 2 + u) * 4096, offF);
  Kg = ld_frag(kh, offF);
#pragma unroll
  for (int u = 0; u < 5; ++u) V[u] = ld_rows(vh + clampt(t0 - 2 + u) * 4096, offR);
  Vg = ld_rows(vh, offR);
#pragma unroll
  for (int t = 1; t < NT; ++t) {
    Q[t] = ld_frag(qh + (t0 + t) * 4096, offF);
    K[t + 4] = ld_frag(kh + clampt(t0 + t + 2) * 4096, offF);
    V[t + 4] = ld_rows(vh + clampt(t0 + t + 2) * 4096, offR);
  }
  Qg = ld_frag(qh, offF);

  const int w = *pw, ng = *png;
  const float NEG = -__builtin_inff();
  f32x4 bias[5], biasG;
  const int e = c - 4 * g;
#pragma unroll
  for (int u = 0; u < 5; ++u)
#pragma unroll
    for (int r = 0; r < 4; ++r) { const int dist = 16 * (2 - u) + e - r; bias[u][r] = (dist <= w && dist >= -w) ? 0.f : NEG; }
#pragma unroll
  for (int r = 0; r < 4; ++r) biasG[r] = (4 * g + r < ng) ? 0.f : NEG;

  VT Vt[NT + 4], Vgt;
#pragma unroll
  for (int t = 0; t < NT; ++t) {
    const int T = t0 + t;
    f32x4 s[6];
#pragma unroll
    for (int u = 0; u < 5; ++u) {
      f32x4 b = bias[u];
      const int kt = T - 2 + u;
      if (kt <= 0 || kt >= NTILE) {
#pragma unroll
        for (int r = 0; r < 4; ++r) b[r] = (kt == 0 && 4 * g + r >= ng) ? b[r] : NEG;
      }
      s[u] = qk(K[t + u], Q[t], b);
    }
    s[5] = qk(Kg, Q[t], biasG);
    float m = NEG;
#pragma unroll
    for (int u = 0; u < 6; ++u)
#pragma unroll
      for (int r = 0; r < 4; ++r) m = fmaxf(m, s[u][r]);
    m = xg_max(m);
    const float mc = -m * C2;
    float l = 0.f;
#pragma unroll
    for (int u = 0; u < 6; ++u)
#pragma unroll
      for (int r = 0; r < 4; ++r) { s[u][r] = __builtin_amdgcn_exp2f(fmaf(s[u][r], C2, mc)); l += s[u][r]; }
    l = xg_sum(l);
    const float inv = 1.f / l;
    u32x2 p[6];
#pragma unroll
    for (int u = 0; u < 6; ++u) { p[u][0] = cvtpk(s[u][0] * inv, s[u][1] * inv); p[u][1] = cvtpk(s[u][2] * inv, s[u][3] * inv); }
    if (t == 0) {
#pragma unroll
      for (int u = 0; u < 5; ++u) Vt[u] = transpose_v(V[u]);
      Vgt = transpose_v(Vg);
    } else Vt[t + 4] = transpose_v(V[t + 4]);
    const int a0 = (t & 1) ? 1 : 0, sg = (t & 1) ? 0 : 4;
    f32x4 o[8];
#pragma unroll
    for (int f = 0; f < 8; ++f) {
      f32x4 acc = {0.f, 0.f, 0.f, 0.f};
      acc = mfma(u32x4{p[a0][0], p[a0][1], p[a0 + 1][0], p[a0 + 1][1]}, u32x4{Vt[t + a0].f[f][0], Vt[t + a0].f[f][1], Vt[t + a0 + 1].f[f][0], Vt[t + a0 + 1].f[f][1]}, acc);
      acc = mfma(u32x4{p[a0 + 2][0], p[a0 + 2][1], p[a0 + 3][0], p[a0 + 3][1]}, u32x4{Vt[t + a0 + 2].f[f][0], Vt[t + a0 + 2].f[f][1], Vt[t + a0 + 3].f[f][0], Vt[t + a0 + 3].f[f][1]}, acc);
      acc = mfma(u32x4{p[sg][0], p[sg][1], p[5][0], p[5][1]}, u32x4{Vt[t + sg].f[f][0], Vt[t + sg].f[f][1], Vgt.f[f][0], Vgt.f[f][1]}, acc);
      o[f] = acc;
    }
#pragma unroll
    for (int r = 0; r < 4; ++r) {
      u32x4 st = {cvtpk(o[0][r], o[1][r]), cvtpk(o[2][r], o[3][r]), cvtpk(o[4][r], o[5][r]), cvtpk(o[6][r], o[7][r])};
      *(u32x4*)(oh + T * 4096 + offR + r * 256) = st;
    }
  }
  {
    f32x4 s[NT];
#pragma unroll
    for (int t = 0; t < NT; ++t) s[t] = qk(K[t + 2], Qg, f32x4{0.f, 0.f, 0.f, 0.f});
    float m = NEG;
#pragma unroll
    for (int t = 0; t < NT; ++t)
#pragma unroll
      for (int r = 0; r < 4; ++r) m = fmaxf(m, s[t][r]);
    m = xg_max(m);
    const float mc = -m * C2;
    float l = 0.f;
    u32x2 p[NT];
#pragma unroll
    for (int t = 0; t < NT; ++t) {
#pragma unroll
      for (int r = 0; r < 4; ++r) { s[t][r] = __builtin_amdgcn_exp2f(fmaf(s[t][r], C2, mc)); l += s[t][r]; }
      p[t][0] = cvtpk(s[t][0], s[t][1]); p[t][1] = cvtpk(s[t][2], s[t][3]);
    }
    l = xg_sum(l);
    f32x4 o[8];
#pragma unroll
    for (int f = 0; f < 8; ++f) {
      f32x4 acc = {0.f, 0.f, 0.f, 0.f};
#pragma unroll
      for (int t = 0; t < NT; t += 2)
        acc = mfma(u32x4{p[t][0], p[t][1], p[t + 1][0], p[t + 1][1]}, u32x4{Vt[t + 2].f[f][0], Vt[t + 2].f[f][1], Vt[t + 3].f[f][0], Vt[t + 3].f[f][1]}, acc);
      o[f] = acc;
    }
    float* rec = ws + (size_t)(head * NWAVE_H + wh) * 16 * PREC;
#pragma unroll
    for (int r = 0; r < 4; ++r) {
      const int row = 4 * g + r;
      if (row < ng) {
        *(f32x4*)(rec + row * PREC + 8 * c) = f32x4{o[0][r], o[1][r], o[2][r], o[3][r]};
        *(f32x4*)(rec + row * PREC + 8 * c + 4) = f32x4{o[4][r], o[5][r], o[6][r], o[7][r]};
      }
    }
    if (g == 0 && c < ng) *(f32x2*)(rec + c * PREC + 128) = f32x2{m * C2, l};
  }
}

__global__ __launch_bounds__(64) void attn_combine(const int* __restrict__ png, char* __restrict__ out, const float* __restrict__ ws) {
  const int head = blockIdx.x >> 4, row = blockIdx.x & 15, lane = threadIdx.x;
  if (row >= *png) return;
  const float* rec = ws + (size_t)head * NWAVE_H * 16 * PREC + row * PREC;
  float M = -__builtin_inff();
  f32x2 ml[NWAVE_H], a[NWAVE_H];
#pragma unroll
  for (int i = 0; i < NWAVE_H; ++i) { ml[i] = *(const f32x2*)(rec + i * 16 * PREC + 128); a[i] = *(const f32x2*)(rec + i * 16 * PREC + 2 * lane); }
#pragma unroll
  for (int i = 0; i < NWAVE_H; ++i) M = fmaxf(M, ml[i][0]);
  float L = 0.f, o0 = 0.f, o1 = 0.f;
#pragma unroll
  for (int i = 0; i < NWAVE_H; ++i) { const float wt = __builtin_amdgcn_exp2f(ml[i][0] - M); L = fmaf(wt, ml[i][1], L); o0 = fmaf(wt, a[i][0], o0); o1 = fmaf(wt, a[i][1], o1); }
  const float inv = 1.f / L;
  *(unsigned*)(out + (size_t)head * S * 256 + row * 256 + lane * 4) = cvtpk(o0 * inv, o1 * inv);
}

extern "C" void kernel_launch(void* const* d_in, const int* in_sizes, int n_in, void* d_out, int out_size, void* d_ws, size_t ws_size, hipStream_t stream) {
  attn_main<<<NBLK, 64 * WPB, 0, stream>>>((const char*)d_in[0], (const char*)d_in[1], (const char*)d_in[2], (const int*)d_in[3], (const int*)d_in[4], (char*)d_out, (float*)d_ws);
  attn_combine<<<H * 16, 64, 0, stream>>>((const int*)d_in[4], (char*)d_out, (const float*)d_ws);
}
```

```cpp
#include <hip/hip_runtime.h>
#include <hip/hip_bf16.h>

typedef __attribute__((ext_vector_type(8))) __bf16 bf16x8;
typedef __attribute__((ext_vector_type(2))) __bf16 bf16x2;
typedef __attribute__((ext_vector_type(4))) float f32x4;
typedef __attribute__((ext_vector_type(2))) float f32x2;
typedef __attribute__((ext_vector_type(4))) unsigned u32x4;
typedef __attribute__((ext_vector_type(2))) unsigned u32x2;
typedef __attribute__((ext_vector_type(4))) short s16x4;
#define LAS __attribute__((address_space(3)))
#define GAS __attribute__((address_space(1)))
typedef unsigned long long u64;

constexpr int H = 32, S = 2048;
constexpr int NTILE = S / 16;
constexpr int L_KG = 0, L_VG = 4096, L_K = 8192  , L_V = 81920  ;
constexpr int LDS_BYTES = 163840;
constexpr float C2 = 0.08838834764831845f * 1.4426950408889634f;
constexpr int PREC = 132;
constexpr int WS_HEAD = 128 * 16 + 8 * 16 * PREC * 4;
#define LDNT " nt"

__device__ __forceinline__ unsigned cvtpk(float lo, float hi) {
  f32x2 v = {lo, hi};
  return __builtin_bit_cast(unsigned, __builtin_convertvector(v, bf16x2));
}
__device__ __forceinline__ float xg_max(float x) {
  auto s = __builtin_amdgcn_permlane16_swap(__float_as_uint(x), __float_as_uint(x), false, false);
  x = fmaxf(__uint_as_float(s[0]), __uint_as_float(s[1]));
  auto t = __builtin_amdgcn_permlane32_swap(__float_as_uint(x), __float_as_uint(x), false, false);
  return fmaxf(__uint_as_float(t[0]), __uint_as_float(t[1]));
}
__device__ __forceinline__ float xg_sum(float x) {
  auto s = __builtin_amdgcn_permlane16_swap(__float_as_uint(x), __float_as_uint(x), false, false);
  x = __uint_as_float(s[0]) + __uint_as_float(s[1]);
  auto t = __builtin_amdgcn_permlane32_swap(__float_as_uint(x), __float_as_uint(x), false, false);
  return __uint_as_float(t[0]) + __uint_as_float(t[1]);
}
__device__ __forceinline__ f32x4 mfma(u32x4 a, u32x4 b, f32x4 c) {
  return __builtin_amdgcn_mfma_f32_16x16x32_bf16(__builtin_bit_cast(bf16x8, a), __builtin_bit_cast(bf16x8, b), c, 0, 0, 0);
}
__device__ __forceinline__ u32x4 lds_b128(unsigned a) { return *(const LAS u32x4*)a; }
__device__ __forceinline__ u32x2 lds_tr(unsigned a) { return __builtin_bit_cast(u32x2, __builtin_amdgcn_ds_read_tr16_b64_v4i16((LAS s16x4*)a)); }
__device__ __forceinline__ void dma(unsigned lds, unsigned voff, const char* base) {
  asm volatile("s_mov_b32 m0, %0\n\ts_nop 0\n\tglobal_load_lds_dwordx4 %1, %2" LDNT :: "s"(lds), "v"(voff), "s"(base) : "memory");
}
__device__ __forceinline__ void ld4(u32x4 (&r)[4], unsigned voff, const char* base) {
  asm volatile("global_load_dwordx4 %0, %4, %5" LDNT "\n\tglobal_load_dwordx4 %1, %4, %5 offset:64" LDNT "\n\t"
               "global_load_dwordx4 %2, %4, %5 offset:128" LDNT "\n\tglobal_load_dwordx4 %3, %4, %5 offset:192" LDNT
               : "=&v"(r[0]), "=&v"(r[1]), "=&v"(r[2]), "=&v"(r[3]) : "v"(voff), "s"(base) : "memory");
}
#define WAIT_VM(n) asm volatile("s_waitcnt vmcnt(" #n ")" ::: "memory")
#define TIE4(r) asm volatile("" : "+v"(r[0]), "+v"(r[1]), "+v"(r[2]), "+v"(r[3]))
__device__ __forceinline__ int clo(int t) { return t < 0 ? 0 : t; }
__device__ __forceinline__ int chi(int t) { return t > NTILE - 1 ? NTILE - 1 : t; }
constexpr int SPIN_CAP = 1 << 17;
#define RLX __ATOMIC_RELAXED, __HIP_MEMORY_SCOPE_AGENT

#define ST(i)

__global__ __launch_bounds__(576) void attn_main(const char* __restrict__ q, const char* __restrict__ k, const char* __restrict__ v,
                                                 const int* __restrict__ pw, const int* __restrict__ png, char* __restrict__ out, char* __restrict__ ws) {
  __shared__ __attribute__((aligned(16))) char lds[LDS_BYTES];
  const int bid = blockIdx.x;
  const int head = (bid & 7) * 4 + (bid >> 6), chunk = (bid >> 3) & 7;
  const int wave = __builtin_amdgcn_readfirstlane(threadIdx.x >> 6), lane = threadIdx.x & 63;
  const int c = lane & 15, g = lane >> 4;
  const int T0 = chunk * 16;
  const size_t hb = (size_t)head * S * 256;
  const char* qh = q + hb; const char* kh = k + hb; const char* vh = v + hb; char* oh = out + hb;
  const unsigned lb = (unsigned)(size_t)(LAS char*)lds;
  const unsigned offF = c * 256 + g * 16;
  const unsigned rdK = lb + (c >> 3) * 2048 + (c & 7) * 64 + ((g ^ ((c >> 3) * 3)) * 16);
  const int vr = 4 * g + (c >> 2);
  const unsigned rdV0 = lb + (vr >> 3) * 2048 + (vr & 7) * 64 + (((vr >> 2) & 1) * 32) + (c & 3) * 8;
  const unsigned rdV1 = rdV0 ^ 32;
  const float NEG = -__builtin_inff();
#define STP(i)
  STP(0);

  if (wave < 8) {
    u32x4 kx[4], qa[4], qb[4];
    const int rg = (wave >> 1) & 1, m2 = wave & 1, sub = lane >> 5, row8 = (lane >> 2) & 7, slot = lane & 3;
    const unsigned vo = (8 * rg + row8) * 256 + (2 * m2 + sub) * 64;
    const unsigned voK = vo + ((slot ^ (3 * rg)) * 16), voV = vo + ((slot ^ (2 * (row8 >> 2))) * 16);
    const int wh = wave >> 2;
    if (wave == 0 || wave == 7) ld4(kx, offF, kh + __builtin_amdgcn_readfirstlane(wave == 0 ? clo(T0 - 2) : chi(T0 + 17)) * 4096);
    const unsigned lw = lb + wave * 1024;
    dma(lw + L_K, voK, kh + clo(T0 - 1 + wh) * 4096);
    dma(lw + L_K + 8 * 8192, voK, kh + chi(T0 + 15 + wh) * 4096);
    dma(lw, wh ? voV : voK, wh ? vh : kh);
#pragma unroll
    for (int j = 1; j < 8; ++j) dma(lw + L_K + j * 8192, voK, kh + (T0 - 1 + 2 * j + wh) * 4096);
    dma(lw + L_V, voV, vh + clo(T0 - 2 + wh) * 4096);
    dma(lw + L_V + 9 * 8192, voV, vh + chi(T0 + 16 + wh) * 4096);
    dma(lw + L_V + 1 * 8192, voV, vh + (T0 + wh) * 4096);
    dma(lw + L_V + 8 * 8192, voV, vh + (T0 + 14 + wh) * 4096);
#pragma unroll
    for (int j = 2; j < 8; ++j) dma(lw + L_V + j * 8192, voV, vh + (T0 - 2 + 2 * j + wh) * 4096);
    ld4(qa, offF, qh + (T0 + wave) * 4096);
    ld4(qb, offF, qh + (T0 + 8 + wave) * 4096);
    STP(1);

    const int w = *pw, ng = *png;
    f32x4 bias[5], biasG;
    const int e = c - 4 * g;
#pragma unroll
    for (int u = 0; u < 5; ++u)
#pragma unroll
      for (int r = 0; r < 4; ++r) { const int dist = 16 * (2 - u) + e - r; bias[u][r] = (dist <= w && dist >= -w) ? 0.f : NEG; }
#pragma unroll
    for (int r = 0; r < 4; ++r) biasG[r] = (4 * g + r < ng) ? 0.f : NEG;

    WAIT_VM(18); __builtin_amdgcn_s_barrier();
    STP(2);
    WAIT_VM(8); __builtin_amdgcn_s_barrier();
    STP(3);
    TIE4(kx);

    auto stage = [&](const int ST, u32x4 (&Q)[4]) {
      const int t = wave + 8 * ST, T = T0 + t;
      const unsigned kb = rdK + L_K + (t - 1) * 4096;
      f32x4 s[6];
#pragma unroll
      for (int u = 0; u < 5; ++u) {
        f32x4 b = bias[u];
        const int kt = T - 2 + u;
        if (kt <= 0 || kt >= NTILE) {
#pragma unroll
          for (int r = 0; r < 4; ++r) b[r] = (kt == 0 && 4 * g + r >= ng) ? b[r] : NEG;
        }
        u32x4 kf[4];
        if ((ST == 0 && u == 0 && wave == 0) || (ST == 1 && u == 4 && wave == 7)) {
#pragma unroll
          for (int i = 0; i < 4; ++i) kf[i] = kx[i];
        } else {
#pragma unroll
          for (int i = 0; i < 4; ++i) kf[i] = lds_b128(kb + u * 4096 + i * 512);
        }
#pragma unroll
        for (int i = 0; i < 4; ++i) b = mfma(kf[i], Q[i], b);
        s[u] = b;
      }
      {
        f32x4 b = biasG;
#pragma unroll
        for (int i = 0; i < 4; ++i) b = mfma(lds_b128(rdK + L_KG + i * 512), Q[i], b);
        s[5] = b;
      }
      float m = NEG;
#pragma unroll
      for (int u = 0; u < 6; ++u)
#pragma unroll
        for (int r = 0; r < 4; ++r) m = fmaxf(m, s[u][r]);
      m = xg_max(m);
      const float mc = -m * C2;
      float l = 0.f;
#pragma unroll
      for (int u = 0; u < 6; ++u)
#pragma unroll
        for (int r = 0; r < 4; ++r) { s[u][r] = __builtin_amdgcn_exp2f(fmaf(s[u][r], C2, mc)); l += s[u][r]; }
      l = xg_sum(l);
      const float inv = __builtin_amdgcn_rcpf(l);
      u32x4 p[3];
#pragma unroll
      for (int u = 0; u < 3; ++u)
        p[u] = u32x4{cvtpk(s[2 * u][0] * inv, s[2 * u][1] * inv), cvtpk(s[2 * u][2] * inv, s[2 * u][3] * inv),
                     cvtpk(s[2 * u + 1][0] * inv, s[2 * u + 1][1] * inv), cvtpk(s[2 * u + 1][2] * inv, s[2 * u + 1][3] * inv)};
      const unsigned vb0 = rdV0 + L_V + t * 4096, vb1 = rdV1 + L_V + t * 4096;
      u32x2 d[8];
#pragma unroll
      for (int f = 0; f < 8; ++f) {
        const unsigned vb = ((f & 1) ? vb1 : vb0) + (f >> 1) * 512, vg = ((f & 1) ? rdV1 : rdV0) + L_VG + (f >> 1) * 512;
        const u32x2 a0 = lds_tr(vb), a1 = lds_tr(vb + 4096), a2 = lds_tr(vb + 8192), a3 = lds_tr(vb + 12288), a4 = lds_tr(vb + 16384), a5 = lds_tr(vg);
        f32x4 acc = {0.f, 0.f, 0.f, 0.f};
        acc = mfma(u32x4{a0[0], a0[1], a1[0], a1[1]}, p[0], acc);
        acc = mfma(u32x4{a2[0], a2[1], a3[0], a3[1]}, p[1], acc);
        acc = mfma(u32x4{a4[0], a4[1], a5[0], a5[1]}, p[2], acc);
        d[f] = u32x2{cvtpk(acc[0], acc[1]), cvtpk(acc[2], acc[3])};
      }
      u32x4 E[4];
#pragma unroll
      for (int mm = 0; mm < 4; ++mm) {
        auto x0 = __builtin_amdgcn_permlane16_swap(d[2 * mm][0], d[2 * mm + 1][0], false, false);
        auto x1 = __builtin_amdgcn_permlane16_swap(d[2 * mm][1], d[2 * mm + 1][1], false, false);
        E[mm] = u32x4{x0[0], x1[0], x0[1], x1[1]};
      }
      char* op = oh + T * 4096 + (c & 7) * 256 + (c >> 3) * 64 + (2 * (g & 1) + (g >> 1)) * 16;
#pragma unroll
      for (int L = 0; L < 2; ++L) {
        u32x4 y0, y1;
#pragma unroll
        for (int i = 0; i < 4; ++i) {
          y0[i] = __builtin_amdgcn_update_dpp(E[2 * L][i], E[2 * L + 1][i], 0x128, 0xf, 0xc, false);
          y1[i] = __builtin_amdgcn_update_dpp(E[2 * L + 1][i], E[2 * L][i], 0x128, 0xf, 0x3, false);
        }
        if (ST == 1 || T != 0 || (c & 7) >= ng) __builtin_nontemporal_store(y0, (u32x4*)(op + L * 128));
        if (ST == 1 || T != 0 || (c & 7) + 8 >= ng) __builtin_nontemporal_store(y1, (u32x4*)(op + L * 128 + 2048));
      }
    };
    WAIT_VM(4); TIE4(qa);
    STP(4);
    stage(0, qa);
    STP(5);
    if (T0 + wave == 0) WAIT_VM(0); else WAIT_VM(4);
    TIE4(qb);
    STP(6);
    stage(1, qb);
    STP(7);
  } else {
    char* wsh = ws + (size_t)head * WS_HEAD;
    GAS unsigned* beat = (GAS unsigned*)wsh;
    GAS unsigned* flags = (GAS unsigned*)(wsh + 128);
    float* pay = (float*)(wsh + 128 * 16);
    const unsigned ent = __hip_atomic_load(lane < 8 ? flags + 32 * lane : beat, RLX);
    u32x4 qg[4];
#pragma unroll
    for (int i = 0; i < 4; ++i) qg[i] = *(const u32x4*)(qh + offF + i * 64);
    const int ng = *png;
    const unsigned b0 = __builtin_amdgcn_readlane(ent, 8), f0 = __builtin_amdgcn_readlane(ent, chunk);
    STP(1);
    TIE4(qg);
    asm volatile("" :: "s"(b0), "s"(f0) : "memory");
    __builtin_amdgcn_s_barrier();
    asm volatile("" ::: "memory");
    STP(2);
    if (chunk == 0 && lane == 0) __hip_atomic_fetch_add(beat, 1u, RLX);
    f32x4 s[16];
#pragma unroll
    for (int u = 0; u < 16; ++u) {
      f32x4 b = {0.f, 0.f, 0.f, 0.f};
      const unsigned kb = rdK + L_K + (u < 8 ? 0 : 32768);
#pragma unroll
      for (int i = 0; i < 4; ++i) b = mfma(lds_b128(kb + ((u & 7) + 1) * 4096 + i * 512), qg[i], b);
      s[u] = b;
    }
    float m = NEG;
#pragma unroll
    for (int u = 0; u < 16; ++u)
#pragma unroll
      for (int r = 0; r < 4; ++r) m = fmaxf(m, s[u][r]);
    m = xg_max(m);
    const float mc = -m * C2;
    float l = 0.f;
#pragma unroll
    for (int u = 0; u < 16; ++u)
#pragma unroll
      for (int r = 0; r < 4; ++r) { s[u][r] = __builtin_amdgcn_exp2f(fmaf(s[u][r], C2, mc)); l += s[u][r]; }
    l = xg_sum(l);
    u32x4 p[8];
#pragma unroll
    for (int u = 0; u < 8; ++u)
      p[u] = u32x4{cvtpk(s[2 * u][0], s[2 * u][1]), cvtpk(s[2 * u][2], s[2 * u][3]), cvtpk(s[2 * u + 1][0], s[2 * u + 1][1]), cvtpk(s[2 * u + 1][2], s[2 * u + 1][3])};
    STP(3);
    asm volatile("" ::: "memory");
    __builtin_amdgcn_s_barrier();
    asm volatile("" ::: "memory");
    STP(4);
    const __amdgpu_buffer_rsrc_t rs = __builtin_amdgcn_make_buffer_rsrc(pay, 0, 8 * 16 * PREC * 4, 0x00020000);
    const unsigned myoff = c < ng ? (unsigned)(((chunk * 16 + c) * PREC + 4 * g) * 4) : 0x80000000u;
#pragma unroll 2
    for (int f = 0; f < 8; ++f) {
      const unsigned vb = ((f & 1) ? rdV1 : rdV0) + L_V + 2 * 4096 + (f >> 1) * 512;
      f32x4 acc = {0.f, 0.f, 0.f, 0.f};
#pragma unroll
      for (int u = 0; u < 8; ++u) {
        const u32x2 a0 = lds_tr(vb + u * 8192), a1 = lds_tr(vb + u * 8192 + 4096);
        acc = mfma(u32x4{a0[0], a0[1], a1[0], a1[1]}, p[u], acc);
      }
      __builtin_amdgcn_raw_buffer_store_b128(__builtin_bit_cast(u32x4, acc), rs, myoff + f * 64, 0, 16);
    }
    __builtin_amdgcn_raw_buffer_store_b64(u32x2{__float_as_uint(m * C2), __float_as_uint(l)}, rs, g == 0 ? myoff + 512 : 0x80000000u, 0, 16);
    STP(5);
    if (chunk != 0) {
      unsigned b1 = __hip_atomic_load(beat, RLX);
      for (int spin = 0; b1 == b0 && spin < SPIN_CAP; ++spin) { __builtin_amdgcn_s_sleep(2); b1 = __hip_atomic_load(beat, RLX); }
      asm volatile("s_waitcnt vmcnt(0)" ::: "memory");
      if (lane == 0) __hip_atomic_store(flags + 32 * chunk, f0 + 1, RLX);
      STP(6); STP(7);
    } else {
      asm volatile("s_waitcnt vmcnt(0)" ::: "memory");
      for (int spin = 0; spin < SPIN_CAP; ++spin) {
        const unsigned cur = __hip_atomic_load(lane < 8 ? flags + 32 * lane : beat, RLX);
        if (__all(lane == 0 || lane >= 8 || cur != ent)) break;
        if (lane == 0) __hip_atomic_fetch_add(beat, 1u, RLX);
        __builtin_amdgcn_s_sleep(1);
      }
      asm volatile("" ::: "memory");
      STP(6);
      for (int r0 = 0; r0 < ng; r0 += 2) {
        const int row = r0 + (lane >> 5);
        const unsigned ro = row < ng ? (unsigned)(row * PREC * 4) : 0x80000000u;
        u32x4 a[8]; u32x2 ml[8];
#pragma unroll
        for (int pp = 0; pp < 8; ++pp) {
          a[pp] = __builtin_amdgcn_raw_buffer_load_b128(rs, ro + pp * 16 * PREC * 4 + (lane & 31) * 16, 0, 16);
          ml[pp] = __builtin_amdgcn_raw_buffer_load_b64(rs, ro + pp * 16 * PREC * 4 + 512, 0, 16);
        }
        float M = NEG;
#pragma unroll
        for (int pp = 0; pp < 8; ++pp) M = fmaxf(M, __uint_as_float(ml[pp][0]));
        float L = 0.f; f32x4 o = {0.f, 0.f, 0.f, 0.f};
#pragma unroll
        for (int pp = 0; pp < 8; ++pp) {
          const float wt = __builtin_amdgcn_exp2f(__uint_as_float(ml[pp][0]) - M);
          L = fmaf(wt, __uint_as_float(ml[pp][1]), L);
          o += wt * __builtin_bit_cast(f32x4, a[pp]);
        }
        const float inv = __builtin_amdgcn_rcpf(L);
        if (row < ng) *(u32x2*)(oh + row * 256 + (lane & 31) * 8) = u32x2{cvtpk(o[0] * inv, o[1] * inv), cvtpk(o[2] * inv, o[3] * inv)};
      }
      STP(7);
    }
  }
}

extern "C" void kernel_launch(void* const* d_in, const int* in_sizes, int n_in, void* d_out, int out_size, void* d_ws, size_t ws_size, hipStream_t stream) {
  attn_main<<<256, 576, 0, stream>>>((const char*)d_in[0], (const char*)d_in[1], (const char*)d_in[2], (const int*)d_in[3], (const int*)d_in[4], (char*)d_out, (char*)d_ws);
}
```

```cpp
#define DIAG_BID 0
#define DIAG_W 0
#define DIAG_A 6
#define DIAG_B 7
#include <hip/hip_runtime.h>
#include <hip/hip_bf16.h>

typedef __attribute__((ext_vector_type(8))) __bf16 bf16x8;
typedef __attribute__((ext_vector_type(2))) __bf16 bf16x2;
typedef __attribute__((ext_vector_type(4))) float f32x4;
typedef __attribute__((ext_vector_type(2))) float f32x2;
typedef __attribute__((ext_vector_type(4))) unsigned u32x4;
typedef __attribute__((ext_vector_type(2))) unsigned u32x2;
typedef __attribute__((ext_vector_type(4))) short s16x4;
#define LAS __attribute__((address_space(3)))
#define GAS __attribute__((address_space(1)))
typedef unsigned long long u64;

constexpr int H = 32, S = 2048;
constexpr int NTILE = S / 16;
constexpr int L_KG = 0, L_VG = 4096, L_K = 8192  , L_V = 81920  ;
constexpr int LDS_BYTES = 163840;
constexpr float C2 = 0.08838834764831845f * 1.4426950408889634f;
constexpr int PREC = 132;
constexpr int WS_HEAD = 128 * 16 + 8 * 16 * PREC * 4;
#define LDNT " nt"

__device__ __forceinline__ unsigned cvtpk(float lo, float hi) {
  f32x2 v = {lo, hi};
  return __builtin_bit_cast(unsigned, __builtin_convertvector(v, bf16x2));
}
__device__ __forceinline__ float xg_max(float x) {
  auto s = __builtin_amdgcn_permlane16_swap(__float_as_uint(x), __float_as_uint(x), false, false);
  x = fmaxf(__uint_as_float(s[0]), __uint_as_float(s[1]));
  auto t = __builtin_amdgcn_permlane32_swap(__float_as_uint(x), __float_as_uint(x), false, false);
  return fmaxf(__uint_as_float(t[0]), __uint_as_float(t[1]));
}
__device__ __forceinline__ float xg_sum(float x) {
  auto s = __builtin_amdgcn_permlane16_swap(__float_as_uint(x), __float_as_uint(x), false, false);
  x = __uint_as_float(s[0]) + __uint_as_float(s[1]);
  auto t = __builtin_amdgcn_permlane32_swap(__float_as_uint(x), __float_as_uint(x), false, false);
  return __uint_as_float(t[0]) + __uint_as_float(t[1]);
}
__device__ __forceinline__ f32x4 mfma(u32x4 a, u32x4 b, f32x4 c) {
  return __builtin_amdgcn_mfma_f32_16x16x32_bf16(__builtin_bit_cast(bf16x8, a), __builtin_bit_cast(bf16x8, b), c, 0, 0, 0);
}
__device__ __forceinline__ u32x4 lds_b128(unsigned a) { return *(const LAS u32x4*)a; }
__device__ __forceinline__ u32x2 lds_tr(unsigned a) { return __builtin_bit_cast(u32x2, __builtin_amdgcn_ds_read_tr16_b64_v4i16((LAS s16x4*)a)); }
__device__ __forceinline__ void dma(unsigned lds, unsigned voff, const char* base) {
  asm volatile("s_mov_b32 m0, %0\n\ts_nop 0\n\tglobal_load_lds_dwordx4 %1, %2" LDNT :: "s"(lds), "v"(voff), "s"(base) : "memory");
}
__device__ __forceinline__ void ld4(u32x4 (&r)[4], unsigned voff, const char* base) {
  asm volatile("global_load_dwordx4 %0, %4, %5" LDNT "\n\tglobal_load_dwordx4 %1, %4, %5 offset:64" LDNT "\n\t"
               "global_load_dwordx4 %2, %4, %5 offset:128" LDNT "\n\tglobal_load_dwordx4 %3, %4, %5 offset:192" LDNT
               : "=&v"(r[0]), "=&v"(r[1]), "=&v"(r[2]), "=&v"(r[3]) : "v"(voff), "s"(base) : "memory");
}
#define WAIT_VM(n) asm volatile("s_waitcnt vmcnt(" #n ")" ::: "memory")
#define TIE4(r) asm volatile("" : "+v"(r[0]), "+v"(r[1]), "+v"(r[2]), "+v"(r[3]))
__device__ __forceinline__ int clo(int t) { return t < 0 ? 0 : t; }
__device__ __forceinline__ int chi(int t) { return t > NTILE - 1 ? NTILE - 1 : t; }
constexpr int SPIN_CAP = 1 << 17;
#define RLX __ATOMIC_RELAXED, __HIP_MEMORY_SCOPE_AGENT

#define ST(i)

__global__ __launch_bounds__(576) void attn_main(const char* __restrict__ q, const char* __restrict__ k, const char* __restrict__ v,
                                                 const int* __restrict__ pw, const int* __restrict__ png, char* __restrict__ out, char* __restrict__ ws) {
  __shared__ __attribute__((aligned(16))) char lds[LDS_BYTES];
  const int bid = blockIdx.x;
  const int head = (bid & 7) * 4 + (bid >> 6), chunk = (bid >> 3) & 7;
  const int wave = __builtin_amdgcn_readfirstlane(threadIdx.x >> 6), lane = threadIdx.x & 63;
  const int c = lane & 15, g = lane >> 4;
  const int T0 = chunk * 16;
  const size_t hb = (size_t)head * S * 256;
  const char* qh = q + hb; const char* kh = k + hb; const char* vh = v + hb; char* oh = out + hb;
  const unsigned lb = (unsigned)(size_t)(LAS char*)lds;
  const unsigned offF = c * 256 + g * 16;
  const unsigned rdK = lb + (c >> 3) * 2048 + (c & 7) * 64 + ((g ^ ((c >> 3) * 3)) * 16);
  const int vr = 4 * g + (c >> 2);
  const unsigned rdV0 = lb + (vr >> 3) * 2048 + (vr & 7) * 64 + (((vr >> 2) & 1) * 32) + (c & 3) * 8;
  const unsigned rdV1 = rdV0 ^ 32;
  const float NEG = -__builtin_inff();
  u64 st_[8];
  const u64 c0_ = __builtin_amdgcn_s_memtime();
#define STP(i) st_[i] = __builtin_amdgcn_s_memrealtime()
  STP(0);

  if (wave < 8) {
    u32x4 kx[4], qa[4], qb[4];
    const int rg = (wave >> 1) & 1, m2 = wave & 1, sub = lane >> 5, row8 = (lane >> 2) & 7, slot = lane & 3;
    const unsigned vo = (8 * rg + row8) * 256 + (2 * m2 + sub) * 64;
    const unsigned voK = vo + ((slot ^ (3 * rg)) * 16), voV = vo + ((slot ^ (2 * (row8 >> 2))) * 16);
    const int wh = wave >> 2;
    if (wave == 0 || wave == 7) ld4(kx, offF, kh + __builtin_amdgcn_readfirstlane(wave == 0 ? clo(T0 - 2) : chi(T0 + 17)) * 4096);
    const unsigned lw = lb + wave * 1024;
    dma(lw + L_K, voK, kh + clo(T0 - 1 + wh) * 4096);
    dma(lw + L_K + 8 * 8192, voK, kh + chi(T0 + 15 + wh) * 4096);
    dma(lw, wh ? voV : voK, wh ? vh : kh);
#pragma unroll
    for (int j = 1; j < 8; ++j) dma(lw + L_K + j * 8192, voK, kh + (T0 - 1 + 2 * j + wh) * 4096);
    dma(lw + L_V, voV, vh + clo(T0 - 2 + wh) * 4096);
    dma(lw + L_V + 9 * 8192, voV, vh + chi(T0 + 16 + wh) * 4096);
    dma(lw + L_V + 1 * 8192, voV, vh + (T0 + wh) * 4096);
    dma(lw + L_V + 8 * 8192, voV, vh + (T0 + 14 + wh) * 4096);
#pragma unroll
    for (int j = 2; j < 8; ++j) dma(lw + L_V + j * 8192, voV, vh + (T0 - 2 + 2 * j + wh) * 4096);
    ld4(qa, offF, qh + (T0 + wave) * 4096);
    ld4(qb, offF, qh + (T0 + 8 + wave) * 4096);
    STP(1);

    const int w = *pw, ng = *png;
    f32x4 bias[5], biasG;
    const int e = c - 4 * g;
#pragma unroll
    for (int u = 0; u < 5; ++u)
#pragma unroll
      for (int r = 0; r < 4; ++r) { const int dist = 16 * (2 - u) + e - r; bias[u][r] = (dist <= w && dist >= -w) ? 0.f : NEG; }
#pragma unroll
    for (int r = 0; r < 4; ++r) biasG[r] = (4 * g + r < ng) ? 0.f : NEG;

    WAIT_VM(18); __builtin_amdgcn_s_barrier();
    STP(2);
    WAIT_VM(8); __builtin_amdgcn_s_barrier();
    STP(3);
    TIE4(kx);

    auto stage = [&](const int ST, u32x4 (&Q)[4]) {
      const int t = wave + 8 * ST, T = T0 + t;
      const unsigned kb = rdK + L_K + (t - 1) * 4096;
      f32x4 s[6];
#pragma unroll
      for (int u = 0; u < 5; ++u) {
        f32x4 b = bias[u];
        const int kt = T - 2 + u;
        if (kt <= 0 || kt >= NTILE) {
#pragma unroll
          for (int r = 0; r < 4; ++r) b[r] = (kt == 0 && 4 * g + r >= ng) ? b[r] : NEG;
        }
        u32x4 kf[4];
        if ((ST == 0 && u == 0 && wave == 0) || (ST == 1 && u == 4 && wave == 7)) {
#pragma unroll
          for (int i = 0; i < 4; ++i) kf[i] = kx[i];
        } else {
#pragma unroll
          for (int i = 0; i < 4; ++i) kf[i] = lds_b128(kb + u * 4096 + i * 512);
        }
#pragma unroll
        for (int i = 0; i < 4; ++i) b = mfma(kf[i], Q[i], b);
        s[u] = b;
      }
      {
        f32x4 b = biasG;
#pragma unroll
        for (int i = 0; i < 4; ++i) b = mfma(lds_b128(rdK + L_KG + i * 512), Q[i], b);
        s[5] = b;
      }
      float m = NEG;
#pragma unroll
      for (int u = 0; u < 6; ++u)
#pragma unroll
        for (int r = 0; r < 4; ++r) m = fmaxf(m, s[u][r]);
      m = xg_max(m);
      const float mc = -m * C2;
      float l = 0.f;
#pragma unroll
      for (int u = 0; u < 6; ++u)
#pragma unroll
        for (int r = 0; r < 4; ++r) { s[u][r] = __builtin_amdgcn_exp2f(fmaf(s[u][r], C2, mc)); l += s[u][r]; }
      l = xg_sum(l);
      const float inv = __builtin_amdgcn_rcpf(l);
      u32x4 p[3];
#pragma unroll
      for (int u = 0; u < 3; ++u)
        p[u] = u32x4{cvtpk(s[2 * u][0] * inv, s[2 * u][1] * inv), cvtpk(s[2 * u][2] * inv, s[2 * u][3] * inv),
                     cvtpk(s[2 * u + 1][0] * inv, s[2 * u + 1][1] * inv), cvtpk(s[2 * u + 1][2] * inv, s[2 * u + 1][3] * inv)};
      const unsigned vb0 = rdV0 + L_V + t * 4096, vb1 = rdV1 + L_V + t * 4096;
      u32x2 d[8];
#pragma unroll
      for (int f = 0; f < 8; ++f) {
        const unsigned vb = ((f & 1) ? vb1 : vb0) + (f >> 1) * 512, vg = ((f & 1) ? rdV1 : rdV0) + L_VG + (f >> 1) * 512;
        const u32x2 a0 = lds_tr(vb), a1 = lds_tr(vb + 4096), a2 = lds_tr(vb + 8192), a3 = lds_tr(vb + 12288), a4 = lds_tr(vb + 16384), a5 = lds_tr(vg);
        f32x4 acc = {0.f, 0.f, 0.f, 0.f};
        acc = mfma(u32x4{a0[0], a0[1], a1[0], a1[1]}, p[0], acc);
        acc = mfma(u32x4{a2[0], a2[1], a3[0], a3[1]}, p[1], acc);
        acc = mfma(u32x4{a4[0], a4[1], a5[0], a5[1]}, p[2], acc);
        d[f] = u32x2{cvtpk(acc[0], acc[1]), cvtpk(acc[2], acc[3])};
      }
      u32x4 E[4];
#pragma unroll
      for (int mm = 0; mm < 4; ++mm) {
        auto x0 = __builtin_amdgcn_permlane16_swap(d[2 * mm][0], d[2 * mm + 1][0], false, false);
        auto x1 = __builtin_amdgcn_permlane16_swap(d[2 * mm][1], d[2 * mm + 1][1], false, false);
        E[mm] = u32x4{x0[0], x1[0], x0[1], x1[1]};
      }
      char* op = oh + T * 4096 + (c & 7) * 256 + (c >> 3) * 64 + (2 * (g & 1) + (g >> 1)) * 16;
#pragma unroll
      for (int L = 0; L < 2; ++L) {
        u32x4 y0, y1;
#pragma unroll
        for (int i = 0; i < 4; ++i) {
          y0[i] = __builtin_amdgcn_update_dpp(E[2 * L][i], E[2 * L + 1][i], 0x128, 0xf, 0xc, false);
          y1[i] = __builtin_amdgcn_update_dpp(E[2 * L + 1][i], E[2 * L][i], 0x128, 0xf, 0x3, false);
        }
        if (ST == 1 || T != 0 || (c & 7) >= ng) __builtin_nontemporal_store(y0, (u32x4*)(op + L * 128));
        if (ST == 1 || T != 0 || (c & 7) + 8 >= ng) __builtin_nontemporal_store(y1, (u32x4*)(op + L * 128 + 2048));
      }
    };
    WAIT_VM(4); TIE4(qa);
    STP(4);
    stage(0, qa);
    STP(5);
    if (T0 + wave == 0) WAIT_VM(0); else WAIT_VM(4);
    TIE4(qb);
    STP(6);
    stage(1, qb);
    STP(7);
  } else {
    char* wsh = ws + (size_t)head * WS_HEAD;
    GAS unsigned* beat = (GAS unsigned*)wsh;
    GAS unsigned* flags = (GAS unsigned*)(wsh + 128);
    float* pay = (float*)(wsh + 128 * 16);
    const unsigned ent = __hip_atomic_load(lane < 8 ? flags + 32 * lane : beat, RLX);
    u32x4 qg[4];
#pragma unroll
    for (int i = 0; i < 4; ++i) qg[i] = *(const u32x4*)(qh + offF + i * 64);
    const int ng = *png;
    const unsigned b0 = __builtin_amdgcn_readlane(ent, 8), f0 = __builtin_amdgcn_readlane(ent, chunk);
    STP(1);
    TIE4(qg);
    asm volatile("" :: "s"(b0), "s"(f0) : "memory");
    __builtin_amdgcn_s_barrier();
    asm volatile("" ::: "memory");
    STP(2);
    if (chunk == 0 && lane == 0) __hip_atomic_fetch_add(beat, 1u, RLX);
    f32x4 s[16];
#pragma unroll
    for (int u = 0; u < 16; ++u) {
      f32x4 b = {0.f, 0.f, 0.f, 0.f};
      const unsigned kb = rdK + L_K + (u < 8 ? 0 : 32768);
#pragma unroll
      for (int i = 0; i < 4; ++i) b = mfma(lds_b128(kb + ((u & 7) + 1) * 4096 + i * 512), qg[i], b);
      s[u] = b;
    }
    float m = NEG;
#pragma unroll
    for (int u = 0; u < 16; ++u)
#pragma unroll
      for (int r = 0; r < 4; ++r) m = fmaxf(m, s[u][r]);
    m = xg_max(m);
    const float mc = -m * C2;
    float l = 0.f;
#pragma unroll
    for (int u = 0; u < 16; ++u)
#pragma unroll
      for (int r = 0; r < 4; ++r) { s[u][r] = __builtin_amdgcn_exp2f(fmaf(s[u][r], C2, mc)); l += s[u][r]; }
    l = xg_sum(l);
    u32x4 p[8];
#pragma unroll
    for (int u = 0; u < 8; ++u)
      p[u] = u32x4{cvtpk(s[2 * u][0], s[2 * u][1]), cvtpk(s[2 * u][2], s[2 * u][3]), cvtpk(s[2 * u + 1][0], s[2 * u + 1][1]), cvtpk(s[2 * u + 1][2], s[2 * u + 1][3])};
    STP(3);
    asm volatile("" ::: "memory");
    __builtin_amdgcn_s_barrier();
    asm volatile("" ::: "memory");
    STP(4);
    const __amdgpu_buffer_rsrc_t rs = __builtin_amdgcn_make_buffer_rsrc(pay, 0, 8 * 16 * PREC * 4, 0x00020000);
    const unsigned myoff = c < ng ? (unsigned)(((chunk * 16 + c) * PREC + 4 * g) * 4) : 0x80000000u;
#pragma unroll 2
    for (int f = 0; f < 8; ++f) {
      const unsigned vb = ((f & 1) ? rdV1 : rdV0) + L_V + 2 * 4096 + (f >> 1) * 512;
      f32x4 acc = {0.f, 0.f, 0.f, 0.f};
#pragma unroll
      for (int u = 0; u < 8; ++u) {
        const u32x2 a0 = lds_tr(vb + u * 8192), a1 = lds_tr(vb + u * 8192 + 4096);
        acc = mfma(u32x4{a0[0], a0[1], a1[0], a1[1]}, p[u], acc);
      }
      __builtin_amdgcn_raw_buffer_store_b128(__builtin_bit_cast(u32x4, acc), rs, myoff + f * 64, 0, 16);
    }
    __builtin_amdgcn_raw_buffer_store_b64(u32x2{__float_as_uint(m * C2), __float_as_uint(l)}, rs, g == 0 ? myoff + 512 : 0x80000000u, 0, 16);
    STP(5);
    if (chunk != 0) {
      unsigned b1 = __hip_atomic_load(beat, RLX);
      for (int spin = 0; b1 == b0 && spin < SPIN_CAP; ++spin) { __builtin_amdgcn_s_sleep(2); b1 = __hip_atomic_load(beat, RLX); }
      asm volatile("s_waitcnt vmcnt(0)" ::: "memory");
      if (lane == 0) __hip_atomic_store(flags + 32 * chunk, f0 + 1, RLX);
      STP(6); STP(7);
    } else {
      asm volatile("s_waitcnt vmcnt(0)" ::: "memory");
      for (int spin = 0; spin < SPIN_CAP; ++spin) {
        const unsigned cur = __hip_atomic_load(lane < 8 ? flags + 32 * lane : beat, RLX);
        if (__all(lane == 0 || lane >= 8 || cur != ent)) break;
        if (lane == 0) __hip_atomic_fetch_add(beat, 1u, RLX);
        __builtin_amdgcn_s_sleep(1);
      }
      asm volatile("" ::: "memory");
      STP(6);
      for (int r0 = 0; r0 < ng; r0 += 2) {
        const int row = r0 + (lane >> 5);
        const unsigned ro = row < ng ? (unsigned)(row * PREC * 4) : 0x80000000u;
        u32x4 a[8]; u32x2 ml[8];
#pragma unroll
        for (int pp = 0; pp < 8; ++pp) {
          a[pp] = __builtin_amdgcn_raw_buffer_load_b128(rs, ro + pp * 16 * PREC * 4 + (lane & 31) * 16, 0, 16);
          ml[pp] = __builtin_amdgcn_raw_buffer_load_b64(rs, ro + pp * 16 * PREC * 4 + 512, 0, 16);
        }
        float M = NEG;
#pragma unroll
        for (int pp = 0; pp < 8; ++pp) M = fmaxf(M, __uint_as_float(ml[pp][0]));
        float L = 0.f; f32x4 o = {0.f, 0.f, 0.f, 0.f};
#pragma unroll
        for (int pp = 0; pp < 8; ++pp) {
          const float wt = __builtin_amdgcn_exp2f(__uint_as_float(ml[pp][0]) - M);
          L = fmaf(wt, __uint_as_float(ml[pp][1]), L);
          o += wt * __builtin_bit_cast(f32x4, a[pp]);
        }
        const float inv = __builtin_amdgcn_rcpf(L);
        if (row < ng) *(u32x2*)(oh + row * 256 + (lane & 31) * 8) = u32x2{cvtpk(o[0] * inv, o[1] * inv), cvtpk(o[2] * inv, o[3] * inv)};
      }
      STP(7);
    }
  }
  if (bid == DIAG_BID && wave == DIAG_W) {
    asm volatile("s_waitcnt vmcnt(0)" ::: "memory");
    const u64 tend = __builtin_amdgcn_s_memrealtime();
    const u64 c1_ = __builtin_amdgcn_s_memtime();
    u64 dt = (DIAG_B == 8 ? tend : st_[DIAG_B & 7]) - st_[DIAG_A];
    u64 spin = dt * 10;
    if (DIAG_B == 9) spin = (c1_ - c0_) * 100 / (tend - st_[0]) * 10;
    if (spin > 100000) spin = 100000;
    while (__builtin_amdgcn_s_memrealtime() - tend < spin) __builtin_amdgcn_s_sleep(1);
  }
}

extern "C" void kernel_launch(void* const* d_in, const int* in_sizes, int n_in, void* d_out, int out_size, void* d_ws, size_t ws_size, hipStream_t stream) {
  attn_main<<<256, 576, 0, stream>>>((const char*)d_in[0], (const char*)d_in[1], (const char*)d_in[2], (const int*)d_in[3], (const int*)d_in[4], (char*)d_out, (char*)d_ws);
}
```

```cpp
#include <hip/hip_runtime.h>
#include <hip/hip_bf16.h>

typedef __attribute__((ext_vector_type(8))) __bf16 bf16x8;
typedef __attribute__((ext_vector_type(2))) __bf16 bf16x2;
typedef __attribute__((ext_vector_type(4))) float f32x4;
typedef __attribute__((ext_vector_type(2))) float f32x2;
typedef __attribute__((ext_vector_type(4))) unsigned u32x4;
typedef __attribute__((ext_vector_type(2))) unsigned u32x2;
typedef __attribute__((ext_vector_type(4))) short s16x4;
#define LAS __attribute__((address_space(3)))
#define GAS __attribute__((address_space(1)))
typedef unsigned long long u64;

constexpr int H = 32, S = 2048;
constexpr int NTILE = S / 16;
constexpr int L_KG = 0, L_VG = 4096, L_K = 8192  , L_V = 81920  ;
constexpr int LDS_BYTES = 163840;
constexpr float C2 = 0.08838834764831845f * 1.4426950408889634f;
constexpr int PREC = 132;
constexpr int WS_HEAD = 128 * 16 + 8 * 16 * PREC * 4;
#define LDNT " nt"

__device__ __forceinline__ unsigned cvtpk(float lo, float hi) {
  f32x2 v = {lo, hi};
  return __builtin_bit_cast(unsigned, __builtin_convertvector(v, bf16x2));
}
__device__ __forceinline__ float xg_max(float x) {
  auto s = __builtin_amdgcn_permlane16_swap(__float_as_uint(x), __float_as_uint(x), false, false);
  x = fmaxf(__uint_as_float(s[0]), __uint_as_float(s[1]));
  auto t = __builtin_amdgcn_permlane32_swap(__float_as_uint(x), __float_as_uint(x), false, false);
  return fmaxf(__uint_as_float(t[0]), __uint_as_float(t[1]));
}
__device__ __forceinline__ float xg_sum(float x) {
  auto s = __builtin_amdgcn_permlane16_swap(__float_as_uint(x), __float_as_uint(x), false, false);
  x = __uint_as_float(s[0]) + __uint_as_float(s[1]);
  auto t = __builtin_amdgcn_permlane32_swap(__float_as_uint(x), __float_as_uint(x), false, false);
  return __uint_as_float(t[0]) + __uint_as_float(t[1]);
}
__device__ __forceinline__ f32x4 mfma(u32x4 a, u32x4 b, f32x4 c) {
  return __builtin_amdgcn_mfma_f32_16x16x32_bf16(__builtin_bit_cast(bf16x8, a), __builtin_bit_cast(bf16x8, b), c, 0, 0, 0);
}
__device__ __forceinline__ u32x4 lds_b128(unsigned a) { return *(const LAS u32x4*)a; }
__device__ __forceinline__ u32x2 lds_tr(unsigned a) { return __builtin_bit_cast(u32x2, __builtin_amdgcn_ds_read_tr16_b64_v4i16((LAS s16x4*)a)); }
__device__ __forceinline__ void dma(unsigned lds, unsigned voff, const char* base) {
  asm volatile("s_mov_b32 m0, %0\n\ts_nop 0\n\tglobal_load_lds_dwordx4 %1, %2" LDNT :: "s"(lds), "v"(voff), "s"(base) : "memory");
}
__device__ __forceinline__ void ld4(u32x4 (&r)[4], unsigned voff, const char* base) {
  asm volatile("global_load_dwordx4 %0, %4, %5" LDNT "\n\tglobal_load_dwordx4 %1, %4, %5 offset:64" LDNT "\n\t"
               "global_load_dwordx4 %2, %4, %5 offset:128" LDNT "\n\tglobal_load_dwordx4 %3, %4, %5 offset:192" LDNT
               : "=&v"(r[0]), "=&v"(r[1]), "=&v"(r[2]), "=&v"(r[3]) : "v"(voff), "s"(base) : "memory");
}
#define WAIT_VM(n) asm volatile("s_waitcnt vmcnt(" #n ")" ::: "memory")
#define TIE4(r) asm volatile("" : "+v"(r[0]), "+v"(r[1]), "+v"(r[2]), "+v"(r[3]))
__device__ __forceinline__ int clo(int t) { return t < 0 ? 0 : t; }
__device__ __forceinline__ int chi(int t) { return t > NTILE - 1 ? NTILE - 1 : t; }
constexpr int SPIN_CAP = 1 << 17;
#define RLX __ATOMIC_RELAXED, __HIP_MEMORY_SCOPE_AGENT

#define ST(i)

__global__ __launch_bounds__(576) void attn_main(const char* __restrict__ q, const char* __restrict__ k, const char* __restrict__ v,
                                                 const int* __restrict__ pw, const int* __restrict__ png, char* __restrict__ out, char* __restrict__ ws) {
  __shared__ __attribute__((aligned(16))) char lds[LDS_BYTES];
  const int bid = blockIdx.x;
  const int head = (bid & 7) * 4 + (bid >> 6), chunk = (bid >> 3) & 7;
  const int wave = __builtin_amdgcn_readfirstlane(threadIdx.x >> 6), lane = threadIdx.x & 63;
  const int c = lane & 15, g = lane >> 4;
  const int T0 = chunk * 16;
  const size_t hb = (size_t)head * S * 256;
  const char* qh = q + hb; const char* kh = k + hb; const char* vh = v + hb; char* oh = out + hb;
  const unsigned lb = (unsigned)(size_t)(LAS char*)lds;
  const unsigned offF = c * 256 + g * 16;
  const unsigned rdK = lb + (c >> 3) * 2048 + (c & 7) * 64 + ((g ^ ((c >> 3) * 3)) * 16);
  const int vr = 4 * g + (c >> 2);
  const unsigned rdV0 = lb + (vr >> 3) * 2048 + (vr & 7) * 64 + (((vr >> 2) & 1) * 32) + (c & 3) * 8;
  const unsigned rdV1 = rdV0 ^ 32;
  const float NEG = -__builtin_inff();
#define STP(i)
  STP(0);

  if (wave < 8) {
    u32x4 kx[4], qa[4], qb[4];
    const int rg = (wave >> 1) & 1, m2 = wave & 1, sub = lane >> 5, row8 = (lane >> 2) & 7, slot = lane & 3;
    const unsigned vo = (8 * rg + row8) * 256 + (2 * m2 + sub) * 64;
    const unsigned voK = vo + ((slot ^ (3 * rg)) * 16), voV = vo + ((slot ^ (2 * (row8 >> 2))) * 16);
    const int wh = wave >> 2;
    if (wave == 0 || wave == 7) ld4(kx, offF, kh + __builtin_amdgcn_readfirstlane(wave == 0 ? clo(T0 - 2) : chi(T0 + 17)) * 4096);
    const unsigned lw = lb + wave * 1024;
    dma(lw + L_K, voK, kh + clo(T0 - 1 + wh) * 4096);
    dma(lw + L_K + 8 * 8192, voK, kh + chi(T0 + 15 + wh) * 4096);
    dma(lw, wh ? voV : voK, wh ? vh : kh);
#pragma unroll
    for (int j = 1; j < 8; ++j) dma(lw + L_K + j * 8192, voK, kh + (T0 - 1 + 2 * j + wh) * 4096);
    dma(lw + L_V, voV, vh + clo(T0 - 2 + wh) * 4096);
    dma(lw + L_V + 9 * 8192, voV, vh + chi(T0 + 16 + wh) * 4096);
    dma(lw + L_V + 1 * 8192, voV, vh + (T0 + wh) * 4096);
    dma(lw + L_V + 8 * 8192, voV, vh + (T0 + 14 + wh) * 4096);
#pragma unroll
    for (int j = 2; j < 8; ++j) dma(lw + L_V + j * 8192, voV, vh + (T0 - 2 + 2 * j + wh) * 4096);
    ld4(qa, offF, qh + (T0 + wave) * 4096);
    ld4(qb, offF, qh + (T0 + 8 + wave) * 4096);
    STP(1);

    const int w = *pw, ng = *png;
    f32x4 bias[5], biasG;
    const int e = c - 4 * g;
#pragma unroll
    for (int u = 0; u < 5; ++u)
#pragma unroll
      for (int r = 0; r < 4; ++r) { const int dist = 16 * (2 - u) + e - r; bias[u][r] = (dist <= w && dist >= -w) ? 0.f : NEG; }
#pragma unroll
    for (int r = 0; r < 4; ++r) biasG[r] = (4 * g + r < ng) ? 0.f : NEG;

    WAIT_VM(18); __builtin_amdgcn_s_barrier();
    STP(2);
    WAIT_VM(8); __builtin_amdgcn_s_barrier();
    STP(3);
    TIE4(kx);

    auto stage = [&](const int ST, u32x4 (&Q)[4]) {
      const int t = wave + 8 * ST, T = T0 + t;
      const unsigned kb = rdK + L_K + (t - 1) * 4096;
      f32x4 s[6];
#pragma unroll
      for (int u = 0; u < 5; ++u) {
        f32x4 b = bias[u];
        const int kt = T - 2 + u;
        if (kt <= 0 || kt >= NTILE) {
#pragma unroll
          for (int r = 0; r < 4; ++r) b[r] = (kt == 0 && 4 * g + r >= ng) ? b[r] : NEG;
        }
        u32x4 kf[4];
        if ((ST == 0 && u == 0 && wave == 0) || (ST == 1 && u == 4 && wave == 7)) {
#pragma unroll
          for (int i = 0; i < 4; ++i) kf[i] = kx[i];
        } else {
#pragma unroll
          for (int i = 0; i < 4; ++i) kf[i] = lds_b128(kb + u * 4096 + i * 512);
        }
#pragma unroll
        for (int i = 0; i < 4; ++i) b = mfma(kf[i], Q[i], b);
        s[u] = b;
      }
      {
        f32x4 b = biasG;
#pragma unroll
        for (int i = 0; i < 4; ++i) b = mfma(lds_b128(rdK + L_KG + i * 512), Q[i], b);
        s[5] = b;
      }
      float m = NEG;
#pragma unroll
      for (int u = 0; u < 6; ++u)
#pragma unroll
        for (int r = 0; r < 4; ++r) m = fmaxf(m, s[u][r]);
      m = xg_max(m);
      const float mc = -m * C2;
      float l = 0.f;
#pragma unroll
      for (int u = 0; u < 6; ++u)
#pragma unroll
        for (int r = 0; r < 4; ++r) { s[u][r] = __builtin_amdgcn_exp2f(fmaf(s[u][r], C2, mc)); l += s[u][r]; }
      l = xg_sum(l);
      const float inv = __builtin_amdgcn_rcpf(l);
      u32x4 p[3];
#pragma unroll
      for (int u = 0; u < 3; ++u)
        p[u] = u32x4{cvtpk(s[2 * u][0] * inv, s[2 * u][1] * inv), cvtpk(s[2 * u][2] * inv, s[2 * u][3] * inv),
                     cvtpk(s[2 * u + 1][0] * inv, s[2 * u + 1][1] * inv), cvtpk(s[2 * u + 1][2] * inv, s[2 * u + 1][3] * inv)};
      const unsigned vb0 = rdV0 + L_V + t * 4096, vb1 = rdV1 + L_V + t * 4096;
      u32x2 d[8];
#pragma unroll
      for (int f = 0; f < 8; ++f) {
        const unsigned vb = ((f & 1) ? vb1 : vb0) + (f >> 1) * 512, vg = ((f & 1) ? rdV1 : rdV0) + L_VG + (f >> 1) * 512;
        const u32x2 a0 = lds_tr(vb), a1 = lds_tr(vb + 4096), a2 = lds_tr(vb + 8192), a3 = lds_tr(vb + 12288), a4 = lds_tr(vb + 16384), a5 = lds_tr(vg);
        f32x4 acc = {0.f, 0.f, 0.f, 0.f};
        acc = mfma(u32x4{a0[0], a0[1], a1[0], a1[1]}, p[0], acc);
        acc = mfma(u32x4{a2[0], a2[1], a3[0], a3[1]}, p[1], acc);
        acc = mfma(u32x4{a4[0], a4[1], a5[0], a5[1]}, p[2], acc);
        d[f] = u32x2{cvtpk(acc[0], acc[1]), cvtpk(acc[2], acc[3])};
      }
      u32x4 E[4];
#pragma unroll
      for (int mm = 0; mm < 4; ++mm) {
        auto x0 = __builtin_amdgcn_permlane16_swap(d[2 * mm][0], d[2 * mm + 1][0], false, false);
        auto x1 = __builtin_amdgcn_permlane16_swap(d[2 * mm][1], d[2 * mm + 1][1], false, false);
        E[mm] = u32x4{x0[0], x1[0], x0[1], x1[1]};
      }
      char* op = oh + T * 4096 + (c & 7) * 256 + (c >> 3) * 64 + (2 * (g & 1) + (g >> 1)) * 16;
#pragma unroll
      for (int L = 0; L < 2; ++L) {
        u32x4 y0, y1;
#pragma unroll
        for (int i = 0; i < 4; ++i) {
          y0[i] = __builtin_amdgcn_update_dpp(E[2 * L][i], E[2 * L + 1][i], 0x128, 0xf, 0xc, false);
          y1[i] = __builtin_amdgcn_update_dpp(E[2 * L + 1][i], E[2 * L][i], 0x128, 0xf, 0x3, false);
        }
        if (ST == 1 || T != 0 || (c & 7) >= ng) asm volatile("global_store_dwordx4 %0, %1, off sc0 sc1\n\ts_nop 1" :: "v"(op + L * 128), "v"(y0) : "memory");
        if (ST == 1 || T != 0 || (c & 7) + 8 >= ng) asm volatile("global_store_dwordx4 %0, %1, off sc0 sc1\n\ts_nop 1" :: "v"(op + L * 128 + 2048), "v"(y1) : "memory");
      }
    };
    WAIT_VM(4); TIE4(qa);
    STP(4);
    stage(0, qa);
    STP(5);
    if (T0 + wave == 0) WAIT_VM(0); else WAIT_VM(4);
    TIE4(qb);
    STP(6);
    stage(1, qb);
    STP(7);
  } else {
    char* wsh = ws + (size_t)head * WS_HEAD;
    GAS unsigned* beat = (GAS unsigned*)wsh;
    GAS unsigned* flags = (GAS unsigned*)(wsh + 128);
    float* pay = (float*)(wsh + 128 * 16);
    const unsigned ent = __hip_atomic_load(lane < 8 ? flags + 32 * lane : beat, RLX);
    u32x4 qg[4];
#pragma unroll
    for (int i = 0; i < 4; ++i) qg[i] = *(const u32x4*)(qh + offF + i * 64);
    const int ng = *png;
    const unsigned b0 = __builtin_amdgcn_readlane(ent, 8), f0 = __builtin_amdgcn_readlane(ent, chunk);
    STP(1);
    TIE4(qg);
    asm volatile("" :: "s"(b0), "s"(f0) : "memory");
    __builtin_amdgcn_s_barrier();
    asm volatile("" ::: "memory");
    STP(2);
    if (chunk == 0 && lane == 0) __hip_atomic_fetch_add(beat, 1u, RLX);
    f32x4 s[16];
#pragma unroll
    for (int u = 0; u < 16; ++u) {
      f32x4 b = {0.f, 0.f, 0.f, 0.f};
      const unsigned kb = rdK + L_K + (u < 8 ? 0 : 32768);
#pragma unroll
      for (int i = 0; i < 4; ++i) b = mfma(lds_b128(kb + ((u & 7) + 1) * 4096 + i * 512), qg[i], b);
      s[u] = b;
    }
    float m = NEG;
#pragma unroll
    for (int u = 0; u < 16; ++u)
#pragma unroll
      for (int r = 0; r < 4; ++r) m = fmaxf(m, s[u][r]);
    m = xg_max(m);
    const float mc = -m * C2;
    float l = 0.f;
#pragma unroll
    for (int u = 0; u < 16; ++u)
#pragma unroll
      for (int r = 0; r < 4; ++r) { s[u][r] = __builtin_amdgcn_exp2f(fmaf(s[u][r], C2, mc)); l += s[u][r]; }
    l = xg_sum(l);
    u32x4 p[8];
#pragma unroll
    for (int u = 0; u < 8; ++u)
      p[u] = u32x4{cvtpk(s[2 * u][0], s[2 * u][1]), cvtpk(s[2 * u][2], s[2 * u][3]), cvtpk(s[2 * u + 1][0], s[2 * u + 1][1]), cvtpk(s[2 * u + 1][2], s[2 * u + 1][3])};
    STP(3);
    asm volatile("" ::: "memory");
    __builtin_amdgcn_s_barrier();
    asm volatile("" ::: "memory");
    STP(4);
    const __amdgpu_buffer_rsrc_t rs = __builtin_amdgcn_make_buffer_rsrc(pay, 0, 8 * 16 * PREC * 4, 0x00020000);
    const unsigned myoff = c < ng ? (unsigned)(((chunk * 16 + c) * PREC + 4 * g) * 4) : 0x80000000u;
#pragma unroll 2
    for (int f = 0; f < 8; ++f) {
      const unsigned vb = ((f & 1) ? rdV1 : rdV0) + L_V + 2 * 4096 + (f >> 1) * 512;
      f32x4 acc = {0.f, 0.f, 0.f, 0.f};
#pragma unroll
      for (int u = 0; u < 8; ++u) {
        const u32x2 a0 = lds_tr(vb + u * 8192), a1 = lds_tr(vb + u * 8192 + 4096);
        acc = mfma(u32x4{a0[0], a0[1], a1[0], a1[1]}, p[u], acc);
      }
      __builtin_amdgcn_raw_buffer_store_b128(__builtin_bit_cast(u32x4, acc), rs, myoff + f * 64, 0, 16);
    }
    __builtin_amdgcn_raw_buffer_store_b64(u32x2{__float_as_uint(m * C2), __float_as_uint(l)}, rs, g == 0 ? myoff + 512 : 0x80000000u, 0, 16);
    STP(5);
    if (chunk != 0) {
      unsigned b1 = __hip_atomic_load(beat, RLX);
      for (int spin = 0; b1 == b0 && spin < SPIN_CAP; ++spin) { __builtin_amdgcn_s_sleep(2); b1 = __hip_atomic_load(beat, RLX); }
      asm volatile("s_waitcnt vmcnt(0)" ::: "memory");
      if (lane == 0) __hip_atomic_store(flags + 32 * chunk, f0 + 1, RLX);
      STP(6); STP(7);
    } else {
      asm volatile("s_waitcnt vmcnt(0)" ::: "memory");
      for (int spin = 0; spin < SPIN_CAP; ++spin) {
        const unsigned cur = __hip_atomic_load(lane < 8 ? flags + 32 * lane : beat, RLX);
        if (__all(lane == 0 || lane >= 8 || cur != ent)) break;
        if (lane == 0) __hip_atomic_fetch_add(beat, 1u, RLX);
        __builtin_amdgcn_s_sleep(1);
      }
      asm volatile("" ::: "memory");
      STP(6);
      for (int r0 = 0; r0 < ng; r0 += 2) {
        const int row = r0 + (lane >> 5);
        const unsigned ro = row < ng ? (unsigned)(row * PREC * 4) : 0x80000000u;
        u32x4 a[8]; u32x2 ml[8];
#pragma unroll
        for (int pp = 0; pp < 8; ++pp) {
          a[pp] = __builtin_amdgcn_raw_buffer_load_b128(rs, ro + pp * 16 * PREC * 4 + (lane & 31) * 16, 0, 16);
          ml[pp] = __builtin_amdgcn_raw_buffer_load_b64(rs, ro + pp * 16 * PREC * 4 + 512, 0, 16);
        }
        float M = NEG;
#pragma unroll
        for (int pp = 0; pp < 8; ++pp) M = fmaxf(M, __uint_as_float(ml[pp][0]));
        float L = 0.f; f32x4 o = {0.f, 0.f, 0.f, 0.f};
#pragma unroll
        for (int pp = 0; pp < 8; ++pp) {
          const float wt = __builtin_amdgcn_exp2f(__uint_as_float(ml[pp][0]) - M);
          L = fmaf(wt, __uint_as_float(ml[pp][1]), L);
          o += wt * __builtin_bit_cast(f32x4, a[pp]);
        }
        const float inv = __builtin_amdgcn_rcpf(L);
        if (row < ng) *(u32x2*)(oh + row * 256 + (lane & 31) * 8) = u32x2{cvtpk(o[0] * inv, o[1] * inv), cvtpk(o[2] * inv, o[3] * inv)};
      }
      STP(7);
    }
  }
}

extern "C" void kernel_launch(void* const* d_in, const int* in_sizes, int n_in, void* d_out, int out_size, void* d_ws, size_t ws_size, hipStream_t stream) {
  attn_main<<<256, 576, 0, stream>>>((const char*)d_in[0], (const char*)d_in[1], (const char*)d_in[2], (const int*)d_in[3], (const int*)d_in[4], (char*)d_out, (char*)d_ws);
}
```

```cpp
#define USE_A 0
#define MODE 0
#include <hip/hip_runtime.h>
#include <hip/hip_bf16.h>

typedef __attribute__((ext_vector_type(8))) __bf16 bf16x8;
typedef __attribute__((ext_vector_type(2))) __bf16 bf16x2;
typedef __attribute__((ext_vector_type(4))) float f32x4;
typedef __attribute__((ext_vector_type(2))) float f32x2;
typedef __attribute__((ext_vector_type(4))) unsigned u32x4;
typedef __attribute__((ext_vector_type(2))) unsigned u32x2;
typedef __attribute__((ext_vector_type(4))) short s16x4;
#define LAS __attribute__((address_space(3)))
#define GAS __attribute__((address_space(1)))
typedef unsigned long long u64;

constexpr int H = 32, S = 2048;
constexpr int NTILE = S / 16;
constexpr int L_KG = 0, L_VG = 4096, L_K = 8192  , L_V = 81920  ;
constexpr int LDS_BYTES = 163840;
constexpr float C2 = 0.08838834764831845f * 1.4426950408889634f;
constexpr int PREC = 132;
constexpr int WS_HEAD = 128 * 16 + 8 * 16 * PREC * 4;
#define LDNT " nt"

__device__ __forceinline__ unsigned cvtpk(float lo, float hi) {
  f32x2 v = {lo, hi};
  return __builtin_bit_cast(unsigned, __builtin_convertvector(v, bf16x2));
}
__device__ __forceinline__ float xg_max(float x) {
  auto s = __builtin_amdgcn_permlane16_swap(__float_as_uint(x), __float_as_uint(x), false, false);
  x = fmaxf(__uint_as_float(s[0]), __uint_as_float(s[1]));
  auto t = __builtin_amdgcn_permlane32_swap(__float_as_uint(x), __float_as_uint(x), false, false);
  return fmaxf(__uint_as_float(t[0]), __uint_as_float(t[1]));
}
__device__ __forceinline__ float xg_sum(float x) {
  auto s = __builtin_amdgcn_permlane16_swap(__float_as_uint(x), __float_as_uint(x), false, false);
  x = __uint_as_float(s[0]) + __uint_as_float(s[1]);
  auto t = __builtin_amdgcn_permlane32_swap(__float_as_uint(x), __float_as_uint(x), false, false);
  return __uint_as_float(t[0]) + __uint_as_float(t[1]);
}
__device__ __forceinline__ f32x4 mfma(u32x4 a, u32x4 b, f32x4 c) {
  return __builtin_amdgcn_mfma_f32_16x16x32_bf16(__builtin_bit_cast(bf16x8, a), __builtin_bit_cast(bf16x8, b), c, 0, 0, 0);
}
__device__ __forceinline__ u32x4 lds_b128(unsigned a) { return *(const LAS u32x4*)a; }
__device__ __forceinline__ u32x2 lds_tr(unsigned a) { return __builtin_bit_cast(u32x2, __builtin_amdgcn_ds_read_tr16_b64_v4i16((LAS s16x4*)a)); }
__device__ __forceinline__ void dma(unsigned lds, unsigned voff, const char* base) {
  asm volatile("s_mov_b32 m0, %0\n\ts_nop 0\n\tglobal_load_lds_dwordx4 %1, %2" LDNT :: "s"(lds), "v"(voff), "s"(base) : "memory");
}
__device__ __forceinline__ void ld4(u32x4 (&r)[4], unsigned voff, const char* base) {
  asm volatile("global_load_dwordx4 %0, %4, %5" LDNT "\n\tglobal_load_dwordx4 %1, %4, %5 offset:64" LDNT "\n\t"
               "global_load_dwordx4 %2, %4, %5 offset:128" LDNT "\n\tglobal_load_dwordx4 %3, %4, %5 offset:192" LDNT
               : "=&v"(r[0]), "=&v"(r[1]), "=&v"(r[2]), "=&v"(r[3]) : "v"(voff), "s"(base) : "memory");
}
#define WAIT_VM(n) asm volatile("s_waitcnt vmcnt(" #n ")" ::: "memory")
#define TIE4(r) asm volatile("" : "+v"(r[0]), "+v"(r[1]), "+v"(r[2]), "+v"(r[3]))
__device__ __forceinline__ int clo(int t) { return t < 0 ? 0 : t; }
__device__ __forceinline__ int chi(int t) { return t > NTILE - 1 ? NTILE - 1 : t; }
constexpr int SPIN_CAP = 1 << 17;
#define RLX __ATOMIC_RELAXED, __HIP_MEMORY_SCOPE_AGENT

#define ST(i)

__global__ __launch_bounds__(576) void attn_main(const char* __restrict__ q, const char* __restrict__ k, const char* __restrict__ v,
                                                 const int* __restrict__ pw, const int* __restrict__ png, char* __restrict__ out, char* __restrict__ ws) {
  __shared__ __attribute__((aligned(16))) char lds[LDS_BYTES];
  u64 tfirst; asm volatile("s_memrealtime %0" : "=s"(tfirst)); __builtin_amdgcn_sched_barrier(0);
  const int bid = blockIdx.x;
  const int head = (bid & 7) * 4 + (bid >> 6), chunk = (bid >> 3) & 7;
  const int wave = __builtin_amdgcn_readfirstlane(threadIdx.x >> 6), lane = threadIdx.x & 63;
  const int c = lane & 15, g = lane >> 4;
  const int T0 = chunk * 16;
  const size_t hb = (size_t)head * S * 256;
  const char* qh = q + hb; const char* kh = k + hb; const char* vh = v + hb; char* oh = out + hb;
  const unsigned lb = (unsigned)(size_t)(LAS char*)lds;
  const unsigned offF = c * 256 + g * 16;
  const unsigned rdK = lb + (c >> 3) * 2048 + (c & 7) * 64 + ((g ^ ((c >> 3) * 3)) * 16);
  const int vr = 4 * g + (c >> 2);
  const unsigned rdV0 = lb + (vr >> 3) * 2048 + (vr & 7) * 64 + (((vr >> 2) & 1) * 32) + (c & 3) * 8;
  const unsigned rdV1 = rdV0 ^ 32;
  const float NEG = -__builtin_inff();
#define STP(i)
  STP(0);

  if (wave < 8) {
    u32x4 kx[4], qa[4], qb[4];
    const int rg = (wave >> 1) & 1, m2 = wave & 1, sub = lane >> 5, row8 = (lane >> 2) & 7, slot = lane & 3;
    const unsigned vo = (8 * rg + row8) * 256 + (2 * m2 + sub) * 64;
    const unsigned voK = vo + ((slot ^ (3 * rg)) * 16), voV = vo + ((slot ^ (2 * (row8 >> 2))) * 16);
    const int wh = wave >> 2;
    if (wave == 0 || wave == 7) ld4(kx, offF, kh + __builtin_amdgcn_readfirstlane(wave == 0 ? clo(T0 - 2) : chi(T0 + 17)) * 4096);
    const unsigned lw = lb + wave * 1024;
    dma(lw + L_K, voK, kh + clo(T0 - 1 + wh) * 4096);
    dma(lw + L_K + 8 * 8192, voK, kh + chi(T0 + 15 + wh) * 4096);
    dma(lw, wh ? voV : voK, wh ? vh : kh);
#pragma unroll
    for (int j = 1; j < 8; ++j) dma(lw + L_K + j * 8192, voK, kh + (T0 - 1 + 2 * j + wh) * 4096);
    dma(lw + L_V, voV, vh + clo(T0 - 2 + wh) * 4096);
    dma(lw + L_V + 9 * 8192, voV, vh + chi(T0 + 16 + wh) * 4096);
    dma(lw + L_V + 1 * 8192, voV, vh + (T0 + wh) * 4096);
    dma(lw + L_V + 8 * 8192, voV, vh + (T0 + 14 + wh) * 4096);
#pragma unroll
    for (int j = 2; j < 8; ++j) dma(lw + L_V + j * 8192, voV, vh + (T0 - 2 + 2 * j + wh) * 4096);
    ld4(qa, offF, qh + (T0 + wave) * 4096);
    ld4(qb, offF, qh + (T0 + 8 + wave) * 4096);
    STP(1);

    const int w = *pw, ng = *png;
    f32x4 bias[5], biasG;
    const int e = c - 4 * g;
#pragma unroll
    for (int u = 0; u < 5; ++u)
#pragma unroll
      for (int r = 0; r < 4; ++r) { const int dist = 16 * (2 - u) + e - r; bias[u][r] = (dist <= w && dist >= -w) ? 0.f : NEG; }
#pragma unroll
    for (int r = 0; r < 4; ++r) biasG[r] = (4 * g + r < ng) ? 0.f : NEG;

    WAIT_VM(18); __builtin_amdgcn_s_barrier();
    STP(2);
    WAIT_VM(8); __builtin_amdgcn_s_barrier();
    STP(3);
    TIE4(kx);

    auto stage = [&](const int ST, u32x4 (&Q)[4]) {
      const int t = wave + 8 * ST, T = T0 + t;
      const unsigned kb = rdK + L_K + (t - 1) * 4096;
      f32x4 s[6];
#pragma unroll
      for (int u = 0; u < 5; ++u) {
        f32x4 b = bias[u];
        const int kt = T - 2 + u;
        if (kt <= 0 || kt >= NTILE) {
#pragma unroll
          for (int r = 0; r < 4; ++r) b[r] = (kt == 0 && 4 * g + r >= ng) ? b[r] : NEG;
        }
        u32x4 kf[4];
        if ((ST == 0 && u == 0 && wave == 0) || (ST == 1 && u == 4 && wave == 7)) {
#pragma unroll
          for (int i = 0; i < 4; ++i) kf[i] = kx[i];
        } else {
#pragma unroll
          for (int i = 0; i < 4; ++i) kf[i] = lds_b128(kb + u * 4096 + i * 512);
        }
#pragma unroll
        for (int i = 0; i < 4; ++i) b = mfma(kf[i], Q[i], b);
        s[u] = b;
      }
      {
        f32x4 b = biasG;
#pragma unroll
        for (int i = 0; i < 4; ++i) b = mfma(lds_b128(rdK + L_KG + i * 512), Q[i], b);
        s[5] = b;
      }
      float m = NEG;
#pragma unroll
      for (int u = 0; u < 6; ++u)
#pragma unroll
        for (int r = 0; r < 4; ++r) m = fmaxf(m, s[u][r]);
      m = xg_max(m);
      const float mc = -m * C2;
      float l = 0.f;
#pragma unroll
      for (int u = 0; u < 6; ++u)
#pragma unroll
        for (int r = 0; r < 4; ++r) { s[u][r] = __builtin_amdgcn_exp2f(fmaf(s[u][r], C2, mc)); l += s[u][r]; }
      l = xg_sum(l);
      const float inv = __builtin_amdgcn_rcpf(l);
      u32x4 p[3];
#pragma unroll
      for (int u = 0; u < 3; ++u)
        p[u] = u32x4{cvtpk(s[2 * u][0] * inv, s[2 * u][1] * inv), cvtpk(s[2 * u][2] * inv, s[2 * u][3] * inv),
                     cvtpk(s[2 * u + 1][0] * inv, s[2 * u + 1][1] * inv), cvtpk(s[2 * u + 1][2] * inv, s[2 * u + 1][3] * inv)};
      const unsigned vb0 = rdV0 + L_V + t * 4096, vb1 = rdV1 + L_V + t * 4096;
      u32x2 d[8];
#pragma unroll
      for (int f = 0; f < 8; ++f) {
        const unsigned vb = ((f & 1) ? vb1 : vb0) + (f >> 1) * 512, vg = ((f & 1) ? rdV1 : rdV0) + L_VG + (f >> 1) * 512;
        const u32x2 a0 = lds_tr(vb), a1 = lds_tr(vb + 4096), a2 = lds_tr(vb + 8192), a3 = lds_tr(vb + 12288), a4 = lds_tr(vb + 16384), a5 = lds_tr(vg);
        f32x4 acc = {0.f, 0.f, 0.f, 0.f};
        acc = mfma(u32x4{a0[0], a0[1], a1[0], a1[1]}, p[0], acc);
        acc = mfma(u32x4{a2[0], a2[1], a3[0], a3[1]}, p[1], acc);
        acc = mfma(u32x4{a4[0], a4[1], a5[0], a5[1]}, p[2], acc);
        d[f] = u32x2{cvtpk(acc[0], acc[1]), cvtpk(acc[2], acc[3])};
      }
      u32x4 E[4];
#pragma unroll
      for (int mm = 0; mm < 4; ++mm) {
        auto x0 = __builtin_amdgcn_permlane16_swap(d[2 * mm][0], d[2 * mm + 1][0], false, false);
        auto x1 = __builtin_amdgcn_permlane16_swap(d[2 * mm][1], d[2 * mm + 1][1], false, false);
        E[mm] = u32x4{x0[0], x1[0], x0[1], x1[1]};
      }
      char* op = oh + T * 4096 + (c & 7) * 256 + (c >> 3) * 64 + (2 * (g & 1) + (g >> 1)) * 16;
#pragma unroll
      for (int L = 0; L < 2; ++L) {
        u32x4 y0, y1;
#pragma unroll
        for (int i = 0; i < 4; ++i) {
          y0[i] = __builtin_amdgcn_update_dpp(E[2 * L][i], E[2 * L + 1][i], 0x128, 0xf, 0xc, false);
          y1[i] = __builtin_amdgcn_update_dpp(E[2 * L + 1][i], E[2 * L][i], 0x128, 0xf, 0x3, false);
        }
        if (ST == 1 || T != 0 || (c & 7) >= ng) __builtin_nontemporal_store(y0, (u32x4*)(op + L * 128));
        if (ST == 1 || T != 0 || (c & 7) + 8 >= ng) __builtin_nontemporal_store(y1, (u32x4*)(op + L * 128 + 2048));
      }
    };
    WAIT_VM(4); TIE4(qa);
    STP(4);
    stage(0, qa);
    STP(5);
    if (T0 + wave == 0) WAIT_VM(0); else WAIT_VM(4);
    TIE4(qb);
    STP(6);
    stage(1, qb);
    STP(7);
  } else {
    char* wsh = ws + (size_t)head * WS_HEAD;
    GAS unsigned* beat = (GAS unsigned*)wsh;
    GAS unsigned* flags = (GAS unsigned*)(wsh + 128);
    float* pay = (float*)(wsh + 128 * 16);
    const unsigned ent = __hip_atomic_load(lane < 8 ? flags + 32 * lane : beat, RLX);
    u32x4 qg[4];
#pragma unroll
    for (int i = 0; i < 4; ++i) qg[i] = *(const u32x4*)(qh + offF + i * 64);
    const int ng = *png;
    const unsigned b0 = __builtin_amdgcn_readlane(ent, 8), f0 = __builtin_amdgcn_readlane(ent, chunk);
    STP(1);
    TIE4(qg);
    asm volatile("" :: "s"(b0), "s"(f0) : "memory");
    __builtin_amdgcn_s_barrier();
    asm volatile("" ::: "memory");
    STP(2);
    if (chunk == 0 && lane == 0) __hip_atomic_fetch_add(beat, 1u, RLX);
    f32x4 s[16];
#pragma unroll
    for (int u = 0; u < 16; ++u) {
      f32x4 b = {0.f, 0.f, 0.f, 0.f};
      const unsigned kb = rdK + L_K + (u < 8 ? 0 : 32768);
#pragma unroll
      for (int i = 0; i < 4; ++i) b = mfma(lds_b128(kb + ((u & 7) + 1) * 4096 + i * 512), qg[i], b);
      s[u] = b;
    }
    float m = NEG;
#pragma unroll
    for (int u = 0; u < 16; ++u)
#pragma unroll
      for (int r = 0; r < 4; ++r) m = fmaxf(m, s[u][r]);
    m = xg_max(m);
    const float mc = -m * C2;
    float l = 0.f;
#pragma unroll
    for (int u = 0; u < 16; ++u)
#pragma unroll
      for (int r = 0; r < 4; ++r) { s[u][r] = __builtin_amdgcn_exp2f(fmaf(s[u][r], C2, mc)); l += s[u][r]; }
    l = xg_sum(l);
    u32x4 p[8];
#pragma unroll
    for (int u = 0; u < 8; ++u)
      p[u] = u32x4{cvtpk(s[2 * u][0], s[2 * u][1]), cvtpk(s[2 * u][2], s[2 * u][3]), cvtpk(s[2 * u + 1][0], s[2 * u + 1][1]), cvtpk(s[2 * u + 1][2], s[2 * u + 1][3])};
    STP(3);
    asm volatile("" ::: "memory");
    __builtin_amdgcn_s_barrier();
    asm volatile("" ::: "memory");
    STP(4);
    const __amdgpu_buffer_rsrc_t rs = __builtin_amdgcn_make_buffer_rsrc(pay, 0, 8 * 16 * PREC * 4, 0x00020000);
    const unsigned myoff = c < ng ? (unsigned)(((chunk * 16 + c) * PREC + 4 * g) * 4) : 0x80000000u;
#pragma unroll 2
    for (int f = 0; f < 8; ++f) {
      const unsigned vb = ((f & 1) ? rdV1 : rdV0) + L_V + 2 * 4096 + (f >> 1) * 512;
      f32x4 acc = {0.f, 0.f, 0.f, 0.f};
#pragma unroll
      for (int u = 0; u < 8; ++u) {
        const u32x2 a0 = lds_tr(vb + u * 8192), a1 = lds_tr(vb + u * 8192 + 4096);
        acc = mfma(u32x4{a0[0], a0[1], a1[0], a1[1]}, p[u], acc);
      }
      __builtin_amdgcn_raw_buffer_store_b128(__builtin_bit_cast(u32x4, acc), rs, myoff + f * 64, 0, 16);
    }
    __builtin_amdgcn_raw_buffer_store_b64(u32x2{__float_as_uint(m * C2), __float_as_uint(l)}, rs, g == 0 ? myoff + 512 : 0x80000000u, 0, 16);
    STP(5);
    if (chunk != 0) {
      unsigned b1 = __hip_atomic_load(beat, RLX);
      for (int spin = 0; b1 == b0 && spin < SPIN_CAP; ++spin) { __builtin_amdgcn_s_sleep(2); b1 = __hip_atomic_load(beat, RLX); }
      asm volatile("s_waitcnt vmcnt(0)" ::: "memory");
      if (lane == 0) __hip_atomic_store(flags + 32 * chunk, f0 + 1, RLX);
      STP(6); STP(7);
    } else {
      asm volatile("s_waitcnt vmcnt(0)" ::: "memory");
      for (int spin = 0; spin < SPIN_CAP; ++spin) {
        const unsigned cur = __hip_atomic_load(lane < 8 ? flags + 32 * lane : beat, RLX);
        if (__all(lane == 0 || lane >= 8 || cur != ent)) break;
        if (lane == 0) __hip_atomic_fetch_add(beat, 1u, RLX);
        __builtin_amdgcn_s_sleep(1);
      }
      asm volatile("" ::: "memory");
      STP(6);
      for (int r0 = 0; r0 < ng; r0 += 2) {
        const int row = r0 + (lane >> 5);
        const unsigned ro = row < ng ? (unsigned)(row * PREC * 4) : 0x80000000u;
        u32x4 a[8]; u32x2 ml[8];
#pragma unroll
        for (int pp = 0; pp < 8; ++pp) {
          a[pp] = __builtin_amdgcn_raw_buffer_load_b128(rs, ro + pp * 16 * PREC * 4 + (lane & 31) * 16, 0, 16);
          ml[pp] = __builtin_amdgcn_raw_buffer_load_b64(rs, ro + pp * 16 * PREC * 4 + 512, 0, 16);
        }
        float M = NEG;
#pragma unroll
        for (int pp = 0; pp < 8; ++pp) M = fmaxf(M, __uint_as_float(ml[pp][0]));
        float L = 0.f; f32x4 o = {0.f, 0.f, 0.f, 0.f};
#pragma unroll
        for (int pp = 0; pp < 8; ++pp) {
          const float wt = __builtin_amdgcn_exp2f(__uint_as_float(ml[pp][0]) - M);
          L = fmaf(wt, __uint_as_float(ml[pp][1]), L);
          o += wt * __builtin_bit_cast(f32x4, a[pp]);
        }
        const float inv = __builtin_amdgcn_rcpf(L);
        if (row < ng) *(u32x2*)(oh + row * 256 + (lane & 31) * 8) = u32x2{cvtpk(o[0] * inv, o[1] * inv), cvtpk(o[2] * inv, o[3] * inv)};
      }
      STP(7);
    }
  }
  { asm volatile("s_waitcnt vmcnt(0)" ::: "memory");
    const u64 te = __builtin_amdgcn_s_memrealtime();
    u64* sp = (u64*)(ws + (16 << 20)) + (bid * 9 + wave) * 2;
    if (lane == 0) { sp[0] = tfirst; sp[1] = te; } }
}

__global__ void kA(char* ws) { u64 t; asm volatile("s_memrealtime %0\n\ts_waitcnt lgkmcnt(0)" : "=s"(t)); if (threadIdx.x == 0) *(u64*)(ws + (17 << 20)) = t; }
__global__ void kB(char* ws) {
  u64 t; asm volatile("s_memrealtime %0\n\ts_waitcnt lgkmcnt(0)" : "=s"(t));
  const u64* sp = (const u64*)(ws + (16 << 20));
  u64 mn = ~0ull, mx = 0;
  for (int i = threadIdx.x; i < 256 * 9; i += 64) { mn = sp[2 * i] < mn ? sp[2 * i] : mn; mx = sp[2 * i + 1] > mx ? sp[2 * i + 1] : mx; }
  for (int o = 32; o > 0; o >>= 1) { u64 a = __shfl_xor(mn, o), b = __shfl_xor(mx, o); mn = a < mn ? a : mn; mx = b > mx ? b : mx; }
  const u64 ta = USE_A ? *(const u64*)(ws + (17 << 20)) : 0;
  u64 dt = MODE == 0 ? 0 : MODE == 1 ? t - mx : MODE == 2 ? mx - mn : mn - ta;
  u64 spin = dt * 10; if (spin > 100000) spin = 100000;
  const u64 t1 = __builtin_amdgcn_s_memrealtime();
  while (__builtin_amdgcn_s_memrealtime() - t1 < spin) __builtin_amdgcn_s_sleep(1);
}
extern "C" void kernel_launch(void* const* d_in, const int* in_sizes, int n_in, void* d_out, int out_size, void* d_ws, size_t ws_size, hipStream_t stream) {
  if (USE_A) kA<<<1, 64, 0, stream>>>((char*)d_ws);
  attn_main<<<256, 576, 0, stream>>>((const char*)d_in[0], (const char*)d_in[1], (const char*)d_in[2], (const int*)d_in[3], (const int*)d_in[4], (char*)d_out, (char*)d_ws);
  kB<<<1, 64, 0, stream>>>((char*)d_ws);
}
```

```cpp
#include <hip/hip_runtime.h>
#include <hip/hip_bf16.h>
#define ORDER 1
#define PRIO 1
#define TOUCH 1
#define QPAT 2
#define DELAY 16
#define STAMP
namespace sk {

typedef __attribute__((ext_vector_type(4))) unsigned u32x4;
#define LAS __attribute__((address_space(3)))
typedef unsigned long long u64;
constexpr int S = 2048, NTILE = 128;
constexpr int L_K = 8192, L_V = 81920, LDS_BYTES = 163840;
#define LDNT " nt"
#define STNT " nt"
#define OBAR 0
#define KVPAT 0
#define HALO 1
__device__ __forceinline__ void dma(unsigned lds, unsigned voff, const char* base) {
  asm volatile("s_mov_b32 m0, %0\n\ts_nop 0\n\tglobal_load_lds_dwordx4 %1, %2" LDNT :: "s"(lds), "v"(voff), "s"(base) : "memory");
}
__device__ __forceinline__ void ld4(u32x4 (&r)[4], unsigned voff, const char* base) {
  asm volatile("global_load_dwordx4 %0, %4, %5" LDNT "\n\tglobal_load_dwordx4 %1, %4, %5 offset:128" LDNT "\n\t"
               "global_load_dwordx4 %2, %4, %5 offset:2048" LDNT "\n\tglobal_load_dwordx4 %3, %4, %5 offset:2176" LDNT
               : "=&v"(r[0]), "=&v"(r[1]), "=&v"(r[2]), "=&v"(r[3]) : "v"(voff), "s"(base) : "memory");
}
__device__ __forceinline__ void st4(u32x4 (&r)[4], unsigned voff, char* base) {
  asm volatile("global_store_dwordx4 %4, %0, %5" STNT "\n\tglobal_store_dwordx4 %4, %1, %5 offset:2048" STNT "\n\t"
               "global_store_dwordx4 %4, %2, %5 offset:128" STNT "\n\tglobal_store_dwordx4 %4, %3, %5 offset:2176" STNT "\n\ts_nop 1"
               :: "v"(r[0]), "v"(r[1]), "v"(r[2]), "v"(r[3]), "v"(voff), "s"(base) : "memory");
}
#define WAIT_VM(n) asm volatile("s_waitcnt vmcnt(" #n ")" ::: "memory")
#define TIE4(r) asm volatile("" : "+v"(r[0]), "+v"(r[1]), "+v"(r[2]), "+v"(r[3]))
#define SETPRIO(n) do { if (PRIO) __builtin_amdgcn_s_setprio(n); } while (0)
#define ORDBAR() do { if (OBAR) __builtin_amdgcn_s_barrier(); } while (0)
__device__ __forceinline__ int clo(int t) { return t < 0 ? 0 : t; }
__device__ __forceinline__ int chi(int t) { return t > NTILE - 1 ? NTILE - 1 : t; }
template <int N> __device__ __forceinline__ void delay() { if (N > 0) { __builtin_amdgcn_s_sleep(N > 127 ? 127 : N); } }

__global__ __launch_bounds__(512) void skel_main(const char* __restrict__ q, const char* __restrict__ k, const char* __restrict__ v, char* __restrict__ out, char* __restrict__ ws) {
  __shared__ __attribute__((aligned(16))) char lds[LDS_BYTES];
  u64 tfirst; asm volatile("s_memrealtime %0" : "=s"(tfirst)); __builtin_amdgcn_sched_barrier(0);
  const int bid = blockIdx.x;
  const int head = (bid & 7) * 4 + (bid >> 6), chunk = (bid >> 3) & 7;
  const int wave = __builtin_amdgcn_readfirstlane(threadIdx.x >> 6), lane = threadIdx.x & 63;
  const int c = lane & 15, g = lane >> 4;
  const int T0 = chunk * 16;
  const size_t hb = (size_t)head * S * 256;
  const char* qh = q + hb; const char* kh = k + hb; const char* vh = v + hb; char* oh = out + hb;
  const unsigned lb = (unsigned)(size_t)(LAS char*)lds;
  const unsigned offO = (c & 7) * 256 + (c >> 3) * 64 + (2 * (g & 1) + (g >> 1)) * 16;
  const unsigned offF = QPAT == 1 ? c * 256 + g * 16 : offO;
  u64* stamps = (u64*)(ws + (16 << 20)) + (bid * 8 + wave) * 16;
  u64 st_[10];
#define STP(i) st_[i] = __builtin_amdgcn_s_memrealtime()
  st_[0] = tfirst;
  u32x4 kx[4], qa[4], qb[4];
  const int rg = (wave >> 1) & 1, m2 = wave & 1, sub = lane >> 5, row8 = (lane >> 2) & 7, slot = lane & 3;
  const unsigned vo = (8 * rg + row8) * 256 + (2 * m2 + sub) * 64;
  const unsigned voK = vo + ((slot ^ (3 * rg)) * 16), voV = vo + ((slot ^ (2 * (row8 >> 2))) * 16);
  const int wh = wave >> 2;
  const unsigned lw = lb + wave * 1024;
  auto issueK = [&]() {
    if (HALO) if (wave == 0 || wave == 7) ld4(kx, offF, kh + __builtin_amdgcn_readfirstlane(wave == 0 ? clo(T0 - 2) : chi(T0 + 17)) * 4096);
    dma(lw + L_K, voK, kh + clo(T0 - 1 + wh) * 4096);
    dma(lw + L_K + 8 * 8192, voK, kh + chi(T0 + 15 + wh) * 4096);
    dma(lw, wh ? voV : voK, wh ? vh : kh);
#pragma unroll
    for (int j = 1; j < 8; ++j) dma(lw + L_K + j * 8192, voK, kh + (T0 - 1 + 2 * j + wh) * 4096);
  };
  auto issueV = [&]() {
    dma(lw + L_V, voV, vh + clo(T0 - 2 + wh) * 4096);
    dma(lw + L_V + 9 * 8192, voV, vh + chi(T0 + 16 + wh) * 4096);
    dma(lw + L_V + 1 * 8192, voV, vh + (T0 + wh) * 4096);
    dma(lw + L_V + 8 * 8192, voV, vh + (T0 + 14 + wh) * 4096);
#pragma unroll
    for (int j = 2; j < 8; ++j) dma(lw + L_V + j * 8192, voV, vh + (T0 - 2 + 2 * j + wh) * 4096);
  };
  unsigned tch;
  if (wave == 3) { const char* tp = lane == 0 ? vh + T0 * 4096 : lane == 1 ? qh + T0 * 4096 : lane == 2 ? (const char*)stamps : (const char*)oh + T0 * 4096;
    asm volatile("global_load_dword %0, %1, off" : "=&v"(tch) : "v"(tp) : "memory"); }
  SETPRIO(3); issueK(); STP(1);
  ld4(qa, offF, qh + (T0 + wave) * 4096);
  ld4(qb, offF, qh + (T0 + 8 + wave) * 4096); STP(2); ORDBAR();
  SETPRIO(0); issueV(); STP(3);
  WAIT_VM(10); __builtin_amdgcn_s_barrier(); STP(4);
  TIE4(qa); TIE4(qb);
  delay<DELAY>();
  STP(5);
  WAIT_VM(0); __builtin_amdgcn_s_barrier(); STP(6);
  delay<DELAY / 2>();
  st4(qa, offO, oh + (T0 + wave) * 4096); STP(7);
  delay<DELAY / 2>();
  st4(qb, offO, oh + (T0 + 8 + wave) * 4096); STP(8);
  TIE4(kx);
  asm volatile("" : "+v"(tch));
  asm volatile("s_waitcnt vmcnt(0)" ::: "memory");
  STP(9);
  asm volatile("s_waitcnt lgkmcnt(0)" ::: "memory"); __builtin_amdgcn_s_barrier();
  if (lane == 0) { for (int i = 0; i < 10; ++i) stamps[i] = st_[i]; }
}

constexpr int NWV = 8;
static void launch(void* const* d_in, void* d_out, void* d_ws, hipStream_t stream) { skel_main<<<256, 512, 0, stream>>>((const char*)d_in[0], (const char*)d_in[1], (const char*)d_in[2], (char*)d_out, (char*)d_ws); }
}
#undef STAMP
#undef STP
#undef TOUCH
#undef QPAT
#undef LAS
#undef SETPRIO
#undef STNT
#undef PRIO
#undef OBAR
#undef KVPAT
#undef DELAY
#undef TIE4
#undef ORDBAR
#undef WAIT_VM
#undef HALO
#undef LDNT
#include <hip/hip_runtime.h>
#include <hip/hip_bf16.h>

typedef __attribute__((ext_vector_type(8))) __bf16 bf16x8;
typedef __attribute__((ext_vector_type(2))) __bf16 bf16x2;
typedef __attribute__((ext_vector_type(4))) float f32x4;
typedef __attribute__((ext_vector_type(2))) float f32x2;
typedef __attribute__((ext_vector_type(4))) unsigned u32x4;
typedef __attribute__((ext_vector_type(2))) unsigned u32x2;
typedef __attribute__((ext_vector_type(4))) short s16x4;
#define LAS __attribute__((address_space(3)))
#define GAS __attribute__((address_space(1)))
typedef unsigned long long u64;

constexpr int H = 32, S = 2048;
constexpr int NTILE = S / 16;
constexpr int L_KG = 0, L_VG = 4096, L_K = 8192  , L_V = 81920  ;
constexpr int LDS_BYTES = 163840;
constexpr float C2 = 0.08838834764831845f * 1.4426950408889634f;
constexpr int PREC = 132;
constexpr int WS_HEAD = 128 * 16 + 8 * 16 * PREC * 4;
#define LDNT " nt"

__device__ __forceinline__ unsigned cvtpk(float lo, float hi) {
  f32x2 v = {lo, hi};
  return __builtin_bit_cast(unsigned, __builtin_convertvector(v, bf16x2));
}
__device__ __forceinline__ float xg_max(float x) {
  auto s = __builtin_amdgcn_permlane16_swap(__float_as_uint(x), __float_as_uint(x), false, false);
  x = fmaxf(__uint_as_float(s[0]), __uint_as_float(s[1]));
  auto t = __builtin_amdgcn_permlane32_swap(__float_as_uint(x), __float_as_uint(x), false, false);
  return fmaxf(__uint_as_float(t[0]), __uint_as_float(t[1]));
}
__device__ __forceinline__ float xg_sum(float x) {
  auto s = __builtin_amdgcn_permlane16_swap(__float_as_uint(x), __float_as_uint(x), false, false);
  x = __uint_as_float(s[0]) + __uint_as_float(s[1]);
  auto t = __builtin_amdgcn_permlane32_swap(__float_as_uint(x), __float_as_uint(x), false, false);
  return __uint_as_float(t[0]) + __uint_as_float(t[1]);
}
__device__ __forceinline__ f32x4 mfma(u32x4 a, u32x4 b, f32x4 c) {
  return __builtin_amdgcn_mfma_f32_16x16x32_bf16(__builtin_bit_cast(bf16x8, a), __builtin_bit_cast(bf16x8, b), c, 0, 0, 0);
}
__device__ __forceinline__ u32x4 lds_b128(unsigned a) { return *(const LAS u32x4*)a; }
__device__ __forceinline__ u32x2 lds_tr(unsigned a) { return __builtin_bit_cast(u32x2, __builtin_amdgcn_ds_read_tr16_b64_v4i16((LAS s16x4*)a)); }
__device__ __forceinline__ void dma(unsigned lds, unsigned voff, const char* base) {
  asm volatile("s_mov_b32 m0, %0\n\ts_nop 0\n\tglobal_load_lds_dwordx4 %1, %2" LDNT :: "s"(lds), "v"(voff), "s"(base) : "memory");
}
__device__ __forceinline__ void ld4(u32x4 (&r)[4], unsigned voff, const char* base) {
  asm volatile("global_load_dwordx4 %0, %4, %5" LDNT "\n\tglobal_load_dwordx4 %1, %4, %5 offset:64" LDNT "\n\t"
               "global_load_dwordx4 %2, %4, %5 offset:128" LDNT "\n\tglobal_load_dwordx4 %3, %4, %5 offset:192" LDNT
               : "=&v"(r[0]), "=&v"(r[1]), "=&v"(r[2]), "=&v"(r[3]) : "v"(voff), "s"(base) : "memory");
}
#define WAIT_VM(n) asm volatile("s_waitcnt vmcnt(" #n ")" ::: "memory")
#define TIE4(r) asm volatile("" : "+v"(r[0]), "+v"(r[1]), "+v"(r[2]), "+v"(r[3]))
__device__ __forceinline__ int clo(int t) { return t < 0 ? 0 : t; }
__device__ __forceinline__ int chi(int t) { return t > NTILE - 1 ? NTILE - 1 : t; }
constexpr int SPIN_CAP = 1 << 17;
#define RLX __ATOMIC_RELAXED, __HIP_MEMORY_SCOPE_AGENT

#define ST(i)

__global__ __launch_bounds__(576) void attn_main(const char* __restrict__ q, const char* __restrict__ k, const char* __restrict__ v,
                                                 const int* __restrict__ pw, const int* __restrict__ png, char* __restrict__ out, char* __restrict__ ws) {
  __shared__ __attribute__((aligned(16))) char lds[LDS_BYTES];
  u64 tfirst; asm volatile("s_memrealtime %0\n\ts_waitcnt lgkmcnt(0)" : "=s"(tfirst)); __builtin_amdgcn_sched_barrier(0);
  const int bid = blockIdx.x;
  const int head = (bid & 7) * 4 + (bid >> 6), chunk = (bid >> 3) & 7;
  const int wave = __builtin_amdgcn_readfirstlane(threadIdx.x >> 6), lane = threadIdx.x & 63;
  const int c = lane & 15, g = lane >> 4;
  const int T0 = chunk * 16;
  const size_t hb = (size_t)head * S * 256;
  const char* qh = q + hb; const char* kh = k + hb; const char* vh = v + hb; char* oh = out + hb;
  const unsigned lb = (unsigned)(size_t)(LAS char*)lds;
  const unsigned offF = c * 256 + g * 16;
  const unsigned rdK = lb + (c >> 3) * 2048 + (c & 7) * 64 + ((g ^ ((c >> 3) * 3)) * 16);
  const int vr = 4 * g + (c >> 2);
  const unsigned rdV0 = lb + (vr >> 3) * 2048 + (vr & 7) * 64 + (((vr >> 2) & 1) * 32) + (c & 3) * 8;
  const unsigned rdV1 = rdV0 ^ 32;
  const float NEG = -__builtin_inff();
#define STP(i)
  STP(0);

  if (wave < 8) {
    u32x4 kx[4], qa[4], qb[4];
    const int rg = (wave >> 1) & 1, m2 = wave & 1, sub = lane >> 5, row8 = (lane >> 2) & 7, slot = lane & 3;
    const unsigned vo = (8 * rg + row8) * 256 + (2 * m2 + sub) * 64;
    const unsigned voK = vo + ((slot ^ (3 * rg)) * 16), voV = vo + ((slot ^ (2 * (row8 >> 2))) * 16);
    const int wh = wave >> 2;
    if (wave == 0 || wave == 7) ld4(kx, offF, kh + __builtin_amdgcn_readfirstlane(wave == 0 ? clo(T0 - 2) : chi(T0 + 17)) * 4096);
    const unsigned lw = lb + wave * 1024;
    dma(lw + L_K, voK, kh + clo(T0 - 1 + wh) * 4096);
    dma(lw + L_K + 8 * 8192, voK, kh + chi(T0 + 15 + wh) * 4096);
    dma(lw, wh ? voV : voK, wh ? vh : kh);
#pragma unroll
    for (int j = 1; j < 8; ++j) dma(lw + L_K + j * 8192, voK, kh + (T0 - 1 + 2 * j + wh) * 4096);
    dma(lw + L_V, voV, vh + clo(T0 - 2 + wh) * 4096);
    dma(lw + L_V + 9 * 8192, voV, vh + chi(T0 + 16 + wh) * 4096);
    dma(lw + L_V + 1 * 8192, voV, vh + (T0 + wh) * 4096);
    dma(lw + L_V + 8 * 8192, voV, vh + (T0 + 14 + wh) * 4096);
#pragma unroll
    for (int j = 2; j < 8; ++j) dma(lw + L_V + j * 8192, voV, vh + (T0 - 2 + 2 * j + wh) * 4096);
    ld4(qa, offF, qh + (T0 + wave) * 4096);
    ld4(qb, offF, qh + (T0 + 8 + wave) * 4096);
    STP(1);

    const int w = *pw, ng = *png;
    f32x4 bias[5], biasG;
    const int e = c - 4 * g;
#pragma unroll
    for (int u = 0; u < 5; ++u)
#pragma unroll
      for (int r = 0; r < 4; ++r) { const int dist = 16 * (2 - u) + e - r; bias[u][r] = (dist <= w && dist >= -w) ? 0.f : NEG; }
#pragma unroll
    for (int r = 0; r < 4; ++r) biasG[r] = (4 * g + r < ng) ? 0.f : NEG;

    WAIT_VM(18); __builtin_amdgcn_s_barrier();
    STP(2);
    WAIT_VM(8); __builtin_amdgcn_s_barrier();
    STP(3);
    TIE4(kx);

    auto stage = [&](const int ST, u32x4 (&Q)[4]) {
      const int t = wave + 8 * ST, T = T0 + t;
      const unsigned kb = rdK + L_K + (t - 1) * 4096;
      f32x4 s[6];
#pragma unroll
      for (int u = 0; u < 5; ++u) {
        f32x4 b = bias[u];
        const int kt = T - 2 + u;
        if (kt <= 0 || kt >= NTILE) {
#pragma unroll
          for (int r = 0; r < 4; ++r) b[r] = (kt == 0 && 4 * g + r >= ng) ? b[r] : NEG;
        }
        u32x4 kf[4];
        if ((ST == 0 && u == 0 && wave == 0) || (ST == 1 && u == 4 && wave == 7)) {
#pragma unroll
          for (int i = 0; i < 4; ++i) kf[i] = kx[i];
        } else {
#pragma unroll
          for (int i = 0; i < 4; ++i) kf[i] = lds_b128(kb + u * 4096 + i * 512);
        }
#pragma unroll
        for (int i = 0; i < 4; ++i) b = mfma(kf[i], Q[i], b);
        s[u] = b;
      }
      {
        f32x4 b = biasG;
#pragma unroll
        for (int i = 0; i < 4; ++i) b = mfma(lds_b128(rdK + L_KG + i * 512), Q[i], b);
        s[5] = b;
      }
      float m = NEG;
#pragma unroll
      for (int u = 0; u < 6; ++u)
#pragma unroll
        for (int r = 0; r < 4; ++r) m = fmaxf(m, s[u][r]);
      m = xg_max(m);
      const float mc = -m * C2;
      float l = 0.f;
#pragma unroll
      for (int u = 0; u < 6; ++u)
#pragma unroll
        for (int r = 0; r < 4; ++r) { s[u][r] = __builtin_amdgcn_exp2f(fmaf(s[u][r], C2, mc)); l += s[u][r]; }
      l = xg_sum(l);
      const float inv = __builtin_amdgcn_rcpf(l);
      u32x4 p[3];
#pragma unroll
      for (int u = 0; u < 3; ++u)
        p[u] = u32x4{cvtpk(s[2 * u][0] * inv, s[2 * u][1] * inv), cvtpk(s[2 * u][2] * inv, s[2 * u][3] * inv),
                     cvtpk(s[2 * u + 1][0] * inv, s[2 * u + 1][1] * inv), cvtpk(s[2 * u + 1][2] * inv, s[2 * u + 1][3] * inv)};
      const unsigned vb0 = rdV0 + L_V + t * 4096, vb1 = rdV1 + L_V + t * 4096;
      u32x2 d[8];
#pragma unroll
      for (int f = 0; f < 8; ++f) {
        const unsigned vb = ((f & 1) ? vb1 : vb0) + (f >> 1) * 512, vg = ((f & 1) ? rdV1 : rdV0) + L_VG + (f >> 1) * 512;
        const u32x2 a0 = lds_tr(vb), a1 = lds_tr(vb + 4096), a2 = lds_tr(vb + 8192), a3 = lds_tr(vb + 12288), a4 = lds_tr(vb + 16384), a5 = lds_tr(vg);
        f32x4 acc = {0.f, 0.f, 0.f, 0.f};
        acc = mfma(u32x4{a0[0], a0[1], a1[0], a1[1]}, p[0], acc);
        acc = mfma(u32x4{a2[0], a2[1], a3[0], a3[1]}, p[1], acc);
        acc = mfma(u32x4{a4[0], a4[1], a5[0], a5[1]}, p[2], acc);
        d[f] = u32x2{cvtpk(acc[0], acc[1]), cvtpk(acc[2], acc[3])};
      }
      u32x4 E[4];
#pragma unroll
      for (int mm = 0; mm < 4; ++mm) {
        auto x0 = __builtin_amdgcn_permlane16_swap(d[2 * mm][0], d[2 * mm + 1][0], false, false);
        auto x1 = __builtin_amdgcn_permlane16_swap(d[2 * mm][1], d[2 * mm + 1][1], false, false);
        E[mm] = u32x4{x0[0], x1[0], x0[1], x1[1]};
      }
      char* op = oh + T * 4096 + (c & 7) * 256 + (c >> 3) * 64 + (2 * (g & 1) + (g >> 1)) * 16;
#pragma unroll
      for (int L = 0; L < 2; ++L) {
        u32x4 y0, y1;
#pragma unroll
        for (int i = 0; i < 4; ++i) {
          y0[i] = __builtin_amdgcn_update_dpp(E[2 * L][i], E[2 * L + 1][i], 0x128, 0xf, 0xc, false);
          y1[i] = __builtin_amdgcn_update_dpp(E[2 * L + 1][i], E[2 * L][i], 0x128, 0xf, 0x3, false);
        }
        if (ST == 1 || T != 0 || (c & 7) >= ng) __builtin_nontemporal_store(y0, (u32x4*)(op + L * 128));
        if (ST == 1 || T != 0 || (c & 7) + 8 >= ng) __builtin_nontemporal_store(y1, (u32x4*)(op + L * 128 + 2048));
      }
    };
    WAIT_VM(4); TIE4(qa);
    STP(4);
    stage(0, qa);
    STP(5);
    if (T0 + wave == 0) WAIT_VM(0); else WAIT_VM(4);
    TIE4(qb);
    STP(6);
    stage(1, qb);
    STP(7);
  } else {
    char* wsh = ws + (size_t)head * WS_HEAD;
    GAS unsigned* beat = (GAS unsigned*)wsh;
    GAS unsigned* flags = (GAS unsigned*)(wsh + 128);
    float* pay = (float*)(wsh + 128 * 16);
    const unsigned ent = __hip_atomic_load(lane < 8 ? flags + 32 * lane : beat, RLX);
    u32x4 qg[4];
#pragma unroll
    for (int i = 0; i < 4; ++i) qg[i] = *(const u32x4*)(qh + offF + i * 64);
    const int ng = *png;
    const unsigned b0 = __builtin_amdgcn_readlane(ent, 8), f0 = __builtin_amdgcn_readlane(ent, chunk);
    STP(1);
    TIE4(qg);
    asm volatile("" :: "s"(b0), "s"(f0) : "memory");
    __builtin_amdgcn_s_barrier();
    asm volatile("" ::: "memory");
    STP(2);
    if (chunk == 0 && lane == 0) __hip_atomic_fetch_add(beat, 1u, RLX);
    f32x4 s[16];
#pragma unroll
    for (int u = 0; u < 16; ++u) {
      f32x4 b = {0.f, 0.f, 0.f, 0.f};
      const unsigned kb = rdK + L_K + (u < 8 ? 0 : 32768);
#pragma unroll
      for (int i = 0; i < 4; ++i) b = mfma(lds_b128(kb + ((u & 7) + 1) * 4096 + i * 512), qg[i], b);
      s[u] = b;
    }
    float m = NEG;
#pragma unroll
    for (int u = 0; u < 16; ++u)
#pragma unroll
      for (int r = 0; r < 4; ++r) m = fmaxf(m, s[u][r]);
    m = xg_max(m);
    const float mc = -m * C2;
    float l = 0.f;
#pragma unroll
    for (int u = 0; u < 16; ++u)
#pragma unroll
      for (int r = 0; r < 4; ++r) { s[u][r] = __builtin_amdgcn_exp2f(fmaf(s[u][r], C2, mc)); l += s[u][r]; }
    l = xg_sum(l);
    u32x4 p[8];
#pragma unroll
    for (int u = 0; u < 8; ++u)
      p[u] = u32x4{cvtpk(s[2 * u][0], s[2 * u][1]), cvtpk(s[2 * u][2], s[2 * u][3]), cvtpk(s[2 * u + 1][0], s[2 * u + 1][1]), cvtpk(s[2 * u + 1][2], s[2 * u + 1][3])};
    STP(3);
    asm volatile("" ::: "memory");
    __builtin_amdgcn_s_barrier();
    asm volatile("" ::: "memory");
    STP(4);
    const __amdgpu_buffer_rsrc_t rs = __builtin_amdgcn_make_buffer_rsrc(pay, 0, 8 * 16 * PREC * 4, 0x00020000);
    const unsigned myoff = c < ng ? (unsigned)(((chunk * 16 + c) * PREC + 4 * g) * 4) : 0x80000000u;
#pragma unroll 2
    for (int f = 0; f < 8; ++f) {
      const unsigned vb = ((f & 1) ? rdV1 : rdV0) + L_V + 2 * 4096 + (f >> 1) * 512;
      f32x4 acc = {0.f, 0.f, 0.f, 0.f};
#pragma unroll
      for (int u = 0; u < 8; ++u) {
        const u32x2 a0 = lds_tr(vb + u * 8192), a1 = lds_tr(vb + u * 8192 + 4096);
        acc = mfma(u32x4{a0[0], a0[1], a1[0], a1[1]}, p[u], acc);
      }
      __builtin_amdgcn_raw_buffer_store_b128(__builtin_bit_cast(u32x4, acc), rs, myoff + f * 64, 0, 16);
    }
    __builtin_amdgcn_raw_buffer_store_b64(u32x2{__float_as_uint(m * C2), __float_as_uint(l)}, rs, g == 0 ? myoff + 512 : 0x80000000u, 0, 16);
    STP(5);
    if (chunk != 0) {
      unsigned b1 = __hip_atomic_load(beat, RLX);
      for (int spin = 0; b1 == b0 && spin < SPIN_CAP; ++spin) { __builtin_amdgcn_s_sleep(2); b1 = __hip_atomic_load(beat, RLX); }
      asm volatile("s_waitcnt vmcnt(0)" ::: "memory");
      if (lane == 0) __hip_atomic_store(flags + 32 * chunk, f0 + 1, RLX);
      STP(6); STP(7);
    } else {
      asm volatile("s_waitcnt vmcnt(0)" ::: "memory");
      for (int spin = 0; spin < SPIN_CAP; ++spin) {
        const unsigned cur = __hip_atomic_load(lane < 8 ? flags + 32 * lane : beat, RLX);
        if (__all(lane == 0 || lane >= 8 || cur != ent)) break;
        if (lane == 0) __hip_atomic_fetch_add(beat, 1u, RLX);
        __builtin_amdgcn_s_sleep(1);
      }
      asm volatile("" ::: "memory");
      STP(6);
      for (int r0 = 0; r0 < ng; r0 += 2) {
        const int row = r0 + (lane >> 5);
        const unsigned ro = row < ng ? (unsigned)(row * PREC * 4) : 0x80000000u;
        u32x4 a[8]; u32x2 ml[8];
#pragma unroll
        for (int pp = 0; pp < 8; ++pp) {
          a[pp] = __builtin_amdgcn_raw_buffer_load_b128(rs, ro + pp * 16 * PREC * 4 + (lane & 31) * 16, 0, 16);
          ml[pp] = __builtin_amdgcn_raw_buffer_load_b64(rs, ro + pp * 16 * PREC * 4 + 512, 0, 16);
        }
        float M = NEG;
#pragma unroll
        for (int pp = 0; pp < 8; ++pp) M = fmaxf(M, __uint_as_float(ml[pp][0]));
        float L = 0.f; f32x4 o = {0.f, 0.f, 0.f, 0.f};
#pragma unroll
        for (int pp = 0; pp < 8; ++pp) {
          const float wt = __builtin_amdgcn_exp2f(__uint_as_float(ml[pp][0]) - M);
          L = fmaf(wt, __uint_as_float(ml[pp][1]), L);
          o += wt * __builtin_bit_cast(f32x4, a[pp]);
        }
        const float inv = __builtin_amdgcn_rcpf(L);
        if (row < ng) *(u32x2*)(oh + row * 256 + (lane & 31) * 8) = u32x2{cvtpk(o[0] * inv, o[1] * inv), cvtpk(o[2] * inv, o[3] * inv)};
      }
      STP(7);
    }
  }
  if (threadIdx.x == 0) ((u64*)(ws + (17 << 20)))[blockIdx.x] = tfirst;
}

#define METRIC 0
#define SIDX 0
#define LAST 9
#define NBLK_ 256

__global__ void kB(char* ws) {
  const u64* sp = (const u64*)(ws + (16 << 20));
  const u64* vp = (const u64*)(ws + (17 << 20));
  u64 mn0 = ~0ull, mx0 = 0, mxs = 0, mns = ~0ull, mxl = 0, mv = ~0ull, sum = 0;
  const u64 ref = sp[0];
  for (int i = threadIdx.x; i < NBLK_ * sk::NWV; i += 256) {
    const u64 a = sp[16 * i], b = sp[16 * i + SIDX], c = sp[16 * i + LAST];
    mn0 = a < mn0 ? a : mn0; mx0 = a > mx0 ? a : mx0; mxs = b > mxs ? b : mxs; mns = b < mns ? b : mns; mxl = c > mxl ? c : mxl; sum += b + 100000 - ref;
  }
  mv = vp[threadIdx.x];
  __shared__ u64 red[7][256];
  red[0][threadIdx.x] = mn0; red[1][threadIdx.x] = mx0; red[2][threadIdx.x] = mxs; red[3][threadIdx.x] = mxl; red[4][threadIdx.x] = mv; red[5][threadIdx.x] = sum; red[6][threadIdx.x] = mns;
  __syncthreads();
  for (int o = 128; o > 0; o >>= 1) {
    if (threadIdx.x < o) { const int t = threadIdx.x;
      red[0][t] = min(red[0][t], red[0][t + o]); red[1][t] = max(red[1][t], red[1][t + o]); red[2][t] = max(red[2][t], red[2][t + o]);
      red[3][t] = max(red[3][t], red[3][t + o]); red[4][t] = min(red[4][t], red[4][t + o]); red[5][t] += red[5][t + o]; red[6][t] = min(red[6][t], red[6][t + o]); }
    __syncthreads();
  }
  const u64 t0 = red[0][0];
  u64 all[7] = {0, red[4][0] - t0, red[1][0] - t0, red[2][0] - t0, red[5][0] / (NBLK_ * sk::NWV) + ref - 100000 - t0, red[3][0] - t0, red[6][0] - t0};
  u64 keep = all[1] ^ all[2] ^ all[3] ^ all[4] ^ all[5] ^ all[6];
  asm volatile("" :: "v"(keep));
  u64 spin = all[METRIC] * 10; if (spin > 100000) spin = 100000;
  const u64 t1 = __builtin_amdgcn_s_memrealtime();
  while (__builtin_amdgcn_s_memrealtime() - t1 < spin) __builtin_amdgcn_s_sleep(1);
}
extern "C" void kernel_launch(void* const* d_in, const int* in_sizes, int n_in, void* d_out, int out_size, void* d_ws, size_t ws_size, hipStream_t stream) {
  sk::launch(d_in, d_out, d_ws, stream);
  attn_main<<<256, 576, 0, stream>>>((const char*)d_in[0], (const char*)d_in[1], (const char*)d_in[2], (const int*)d_in[3], (const int*)d_in[4], (char*)d_out, (char*)d_ws);
  kB<<<1, 256, 0, stream>>>((char*)d_ws);
}
```

```cpp
#include <hip/hip_runtime.h>
#include <hip/hip_bf16.h>

namespace {
constexpr int kH = 32, kS = 2048, kD = 128;
typedef __attribute__((ext_vector_type(4))) unsigned u32x4;

__device__ __forceinline__ float bf_lo(unsigned x) { return __uint_as_float(x << 16); }
__device__ __forceinline__ float bf_hi(unsigned x) { return __uint_as_float(x & 0xffff0000u); }

__device__ __forceinline__ void generic_rows(const unsigned* __restrict__ q, const unsigned* __restrict__ k, const unsigned* __restrict__ v,
                                             int window, int ng, unsigned* __restrict__ out, int first, int stride, int lane) {
  const float c2 = 0.08838834764831845f * 1.4426950408889634f;
  const bool none = window < 0 && ng <= 0;
  const int ngc = ng < 0 ? 0 : (ng > kS ? kS : ng);
  const int wc = window < 0 ? -1 : (window > kS ? kS : window);
  for (int row = first; row < kH * kS; row += stride) {
    const int h = row / kS, i = row % kS;
    const unsigned qv = q[(size_t)row * 64 + lane];
    const unsigned* kh = k + (size_t)h * kS * 64;
    const unsigned* vh = v + (size_t)h * kS * 64;
    const bool full = none || i < ngc;
    int lo = i - wc, hi = i + wc + 1;
    lo = lo < ngc ? ngc : lo; hi = hi > kS ? kS : hi;
    const int n0 = full ? kS : ngc, n1 = full ? 0 : (hi > lo ? hi - lo : 0);
    float m = -__builtin_inff(), l = 0.f, a0 = 0.f, a1 = 0.f;
    for (int base = 0; base < n0 + n1; base += 64) {
      const int idx = base + lane;
      const bool ok = idx < n0 + n1;
      const int j = ok ? (idx < n0 ? idx : lo + idx - n0) : 0;
      float sc = 0.f;
      for (int d4 = 0; d4 < 16; ++d4) {
        const u32x4 kk = *(const u32x4*)(kh + (size_t)j * 64 + d4 * 4);
#pragma unroll
        for (int e = 0; e < 4; ++e) {
          const unsigned qq = __shfl(qv, d4 * 4 + e);
          sc = fmaf(bf_lo(qq), bf_lo(kk[e]), sc);
          sc = fmaf(bf_hi(qq), bf_hi(kk[e]), sc);
        }
      }
      sc = ok ? (none ? 0.f : sc * c2) : -__builtin_inff();
      float mx = sc;
      for (int o = 32; o > 0; o >>= 1) mx = fmaxf(mx, __shfl_xor(mx, o));
      const float mn = fmaxf(m, mx), al = exp2f(m - mn), p = exp2f(sc - mn);
      float ps = p;
      for (int o = 32; o > 0; o >>= 1) ps += __shfl_xor(ps, o);
      l = l * al + ps; a0 *= al; a1 *= al; m = mn;
      const int cnt = n0 + n1 - base < 64 ? n0 + n1 - base : 64;
      for (int t = 0; t < cnt; ++t) {
        const float pt = __shfl(p, t);
        const unsigned vv = vh[(size_t)__shfl(j, t) * 64 + lane];
        a0 = fmaf(pt, bf_lo(vv), a0); a1 = fmaf(pt, bf_hi(vv), a1);
      }
    }
    typedef __attribute__((ext_vector_type(2))) float f32x2;
    typedef __attribute__((ext_vector_type(2))) __bf16 bf16x2;
    const f32x2 r = {a0 / l, a1 / l};
    out[(size_t)row * 64 + lane] = __builtin_bit_cast(unsigned, __builtin_convertvector(r, bf16x2));
  }
}
}

__global__ __launch_bounds__(768, 3) void glw_attn_kernel(const unsigned* __restrict__ q, const unsigned* __restrict__ k, const unsigned* __restrict__ v,
                                                       const int* __restrict__ pw, const int* __restrict__ png, unsigned* __restrict__ out, char* __restrict__ ws) {
  __shared__ __attribute__((aligned(16))) char lds[163840];
  const int wave = __builtin_amdgcn_readfirstlane(threadIdx.x >> 6);
  const int bid = blockIdx.x;
#if defined(__HIP_DEVICE_COMPILE__)
  asm volatile(
    "L_begin%=:\n"
    "s_load_dwordx8 s[16:23], %[ka], 0x0\n"
    "s_load_dwordx4 s[24:27], %[ka], 0x20\n"
    "s_load_dwordx2 s[28:29], %[ka], 0x30\n"
    "s_mov_b32 s33, %[wave]\n"
    "s_cmp_eq_u32 s33, 5\n"
    "s_cbranch_scc0 L_nospf%=\n"
    "s_getpc_b64 s[90:91]\n"
    "s_load_dword s92, s[90:91], 0x800\n"
    "s_load_dword s93, s[90:91], 0x1000\n"
    "s_load_dword s94, s[90:91], 0x1800\n"
    "s_load_dword s95, s[90:91], 0x2000\n"
    "s_load_dword s96, s[90:91], 0x2800\n"
    "L_nospf%=:\n"
    "s_and_b32 s35, %[bid], 7\n"
    "s_lshl_b32 s35, s35, 2\n"
    "s_lshr_b32 s13, %[bid], 6\n"
    "s_add_u32 s35, s35, s13\n"
    "s_bfe_u32 s34, %[bid], 0x30003\n"
    "s_lshl_b32 s34, s34, 4\n"
    "v_mbcnt_lo_u32_b32 v47, -1, 0\n"
    "v_mbcnt_hi_u32_b32 v47, -1, v47\n"
    "v_and_b32 v48, 15, v47\n"
    "v_lshrrev_b32 v49, 4, v47\n"
    "s_cmp_ge_u32 s33, 8\n"
    "s_cbranch_scc1 L_helper%=\n"
    "s_setprio 2\n"
    "s_lshr_b32 s12, s34, 2\n"
    "s_and_b32 s12, s12, 4\n"
    "s_xor_b32 s12, s12, s33\n"
    "s_bfe_u32 s14, s33, 0x10001\n"
    "s_and_b32 s15, s33, 1\n"
    "v_bfe_u32 v53, v47, 2, 3\n"
    "v_lshrrev_b32 v54, 5, v47\n"
    "v_lshlrev_b32 v54, 6, v54\n"
    "v_lshl_or_b32 v54, v53, 8, v54\n"
    "s_lshl_b32 s15, s15, 7\n"
    "s_lshl_b32 s13, s14, 11\n"
    "s_or_b32 s15, s15, s13\n"
    "v_or_b32 v54, s15, v54\n"
    "v_and_b32 v50, 3, v47\n"
    "v_xor_b32 v50, s14, v50\n"
    "v_lshl_or_b32 v50, v50, 4, v54\n"
    "s_lshl_b32 s13, s35, 19\n"
    "s_and_b32 s14, s33, 3\n"
    "s_lshl_b32 s14, s14, 10\n"
    "s_and_b32 s15, s33, 4\n"
    "s_lshl_b32 s54, s15, 11\n"
    "s_lshl_b32 s52, s34, 12\n"
    "s_add_u32 s52, s52, s54\n"
    "s_add_u32 s52, s52, -8192\n"
    "s_add_u32 s54, s54, s14\n"
    "s_add_u32 s54, s54, 4096\n"
    "s_lshl_b32 s56, s15, 12\n"
    "s_add_u32 s56, s56, 16384\n"
    "s_add_u32 s58, s54, s56\n"
    "s_add_u32 s56, s56, s52\n"
    "s_bitcmp1_b32 s34, 4\n"
    "s_cselect_b32 s57, 8, 11\n"
    "s_cmp_eq_u32 s15, 0\n"
    "s_cselect_b32 s57, 0, s57\n"
    "s_lshl_b32 s57, s57, 12\n"
    "s_add_u32 s57, s57, s54\n"
    "s_add_u32 m0, s54, 0\n"
    "s_add_u32 s55, s52, 0\n"
    "s_min_u32 s55, s55, 0x40000000\n"
    "v_add_u32 v52, s55, v50\n"
    "s_waitcnt lgkmcnt(0)\n"
    "s_memrealtime s[8:9]\n"
    "s_add_u32 s36, s18, s13\n"
    "s_addc_u32 s37, s19, 0\n"
    "s_and_b32 s37, s37, 0xffff\n"
    "s_mov_b32 s38, 0x80000\n"
    "s_mov_b32 s39, 0x20000\n"
    "buffer_load_dwordx4 v52, s[36:39], 0 offen nt lds\n"
    "s_load_dword s30, s[22:23], 0x0\n"
    "s_load_dword s31, s[24:25], 0x0\n"
    "s_add_u32 m0, s54, 65536\n"
    "s_add_u32 s55, s52, 65536\n"
    "v_add_u32 v52, s55, v50\n"
    "buffer_load_dwordx4 v52, s[36:39], 0 offen nt lds\n"
    "s_add_u32 m0, s58, 0\n"
    "s_add_u32 s55, s56, 0\n"
    "v_add_u32 v52, s55, v50\n"
    "buffer_load_dwordx4 v52, s[36:39], 0 offen nt lds\n"
    "s_add_u32 s44, s16, s13\n"
    "s_addc_u32 s45, s17, 0\n"
    "s_and_b32 s45, s45, 0xffff\n"
    "s_mov_b32 s46, 0x80000\n"
    "s_mov_b32 s47, 0x20000\n"
    "s_add_u32 m0, s58, 4096\n"
    "s_add_u32 s55, s56, 4096\n"
    "v_add_u32 v52, s55, v50\n"
    "buffer_load_dwordx4 v52, s[36:39], 0 offen nt lds\n"
    "s_add_u32 s48, s26, s13\n"
    "s_addc_u32 s49, s27, 0\n"
    "s_and_b32 s49, s49, 0xffff\n"
    "s_mov_b32 s50, 0x80000\n"
    "s_mov_b32 s51, 0x20000\n"
    "s_add_u32 m0, s58, 8192\n"
    "s_add_u32 s55, s56, 8192\n"
    "v_add_u32 v52, s55, v50\n"
    "buffer_load_dwordx4 v52, s[36:39], 0 offen nt lds\n"
    "s_add_u32 s40, s20, s13\n"
    "s_addc_u32 s41, s21, 0\n"
    "s_and_b32 s41, s41, 0xffff\n"
    "s_mov_b32 s42, 0x80000\n"
    "s_mov_b32 s43, 0x20000\n"
    "s_add_u32 m0, s54, 4096\n"
    "s_add_u32 s55, s52, 4096\n"
    "s_min_u32 s55, s55, 0x40000000\n"
    "v_add_u32 v52, s55, v50\n"
    "buffer_load_dwordx4 v52, s[36:39], 0 offen nt lds\n"
    "s_add_u32 m0, s57, 69632\n"
    "s_add_u32 s55, s52, 69632\n"
    "v_add_u32 v52, s55, v50\n"
    "buffer_load_dwordx4 v52, s[36:39], 0 offen nt lds\n"
    "s_add_u32 m0, s58, 12288\n"
    "s_add_u32 s55, s56, 12288\n"
    "v_add_u32 v52, s55, v50\n"
    "buffer_load_dwordx4 v52, s[36:39], 0 offen nt lds\n"
    "s_add_u32 m0, s58, 16384\n"
    "s_add_u32 s55, s56, 16384\n"
    "v_add_u32 v52, s55, v50\n"
    "buffer_load_dwordx4 v52, s[36:39], 0 offen nt lds\n"
    "s_add_u32 m0, s58, 20480\n"
    "s_add_u32 s55, s56, 20480\n"
    "v_add_u32 v52, s55, v50\n"
    "buffer_load_dwordx4 v52, s[36:39], 0 offen nt lds\n"
    "s_setprio 1\n"
    "s_lshl_b32 s13, s12, 1\n"
    "s_add_u32 s26, s34, s13\n"
    "s_lshl_b32 s24, s26, 12\n"
    "s_mul_i32 s14, s33, 8704\n"
    "s_add_u32 s14, s14, 8192\n"
    "s_bitcmp1_b32 s34, 4\n"
    "s_cselect_b32 s20, -1, 1\n"
    "s_cselect_b32 s21, 77824, 0\n"
    "s_cselect_b32 s16, 0, 49152\n"
    "s_lshl_b32 s25, s33, 13\n"
    "s_add_u32 s16, s16, s25\n"
    "s_add_u32 s16, s16, 81920\n"
    "s_lshl_b32 s25, s12, 13\n"
    "s_cmp_eq_u32 s15, 0\n"
    "s_cselect_b32 s16, s16, s14\n"
    "v_bfe_u32 v53, v47, 2, 3\n"
    "v_lshrrev_b32 v54, 5, v47\n"
    "v_lshlrev_b32 v54, 6, v54\n"
    "v_lshl_or_b32 v54, v53, 8, v54\n"
    "v_and_b32 v53, 3, v47\n"
    "v_lshl_or_b32 v157, v53, 4, v54\n"
    "v_add_u32 v157, s24, v157\n"
    "v_xor_b32 v158, 0x810, v157\n"
    "s_bfe_u32 s13, s33, 0x10001\n"
    "s_lshl_b32 s13, s13, 4\n"
    "v_lshlrev_b32 v53, 1, v47\n"
    "v_and_b32 v53, 32, v53\n"
    "v_xor_b32 v53, s13, v53\n"
    "v_xor_b32 v155, v53, v50\n"
    "s_lshl_b32 s13, s34, 12\n"
    "s_add_u32 s13, s13, s21\n"
    "s_add_u32 s13, s13, -8192\n"
    "v_add_u32 v155, s13, v155\n"
    "s_and_b32 s17, s33, 3\n"
    "s_lshl_b32 s17, s17, 10\n"
    "s_add_u32 s17, s17, s21\n"
    "s_add_u32 s17, s17, 81920\n"
    "s_waitcnt lgkmcnt(0)\n"
    "s_cmp_gt_u32 s30, 32\n"
    "s_cbranch_scc1 L_fb%=\n"
    "s_cmp_gt_u32 s31, 4\n"
    "s_cbranch_scc1 L_fb%=\n"
    "s_mov_b32 s13, 659456\n"
    "s_add_u32 s36, s28, s13\n"
    "s_addc_u32 s37, s29, 0\n"
    "s_and_b32 s37, s37, 0xffff\n"
    "s_mov_b32 s38, 1024\n"
    "s_mov_b32 s39, 0x20000\n"
    "s_cmp_eq_u32 s15, 0\n"
    "s_cbranch_scc0 L_snp%=\n"
    "s_setprio 3\n"
    "v_mbcnt_lo_u32_b32 v53, -1, 0\n"
    "v_mbcnt_hi_u32_b32 v53, -1, v53\n"
    "s_and_b32 s13, %[bid], 7\n"
    "s_lshl_b32 s13, s13, 7\n"
    "v_and_b32 v53, 7, v53\n"
    "v_lshl_add_u32 v53, v53, 2, s13\n"
    "buffer_load_dword v167, v53, s[36:39], 0 offen sc1\n"
    "s_setprio 1\n"
    "L_snp%=:\n"
    "v_lshrrev_b32 v53, 3, v48\n"
    "v_mul_u32_u24 v152, 0x810, v53\n"
    "v_and_b32 v53, 7, v48\n"
    "v_lshl_or_b32 v152, v53, 6, v152\n"
    "v_and_b32 v53, 1, v49\n"
    "v_lshl_or_b32 v152, v53, 5, v152\n"
    "v_lshrrev_b32 v54, 1, v49\n"
    "v_lshl_or_b32 v152, v54, 9, v152\n"
    "v_mul_u32_u24 v160, 0x120, v53\n"
    "v_lshl_or_b32 v160, v54, 11, v160\n"
    "v_lshrrev_b32 v53, 2, v48\n"
    "v_lshl_or_b32 v160, v53, 6, v160\n"
    "v_and_b32 v53, 3, v48\n"
    "v_lshl_or_b32 v160, v53, 3, v160\n"
    "v_add_u32 v164, s16, v152\n"
    "s_add_u32 s13, s25, 4096\n"
    "v_add_u32 v152, s13, v152\n"
    "v_xor_b32 v153, 16, v152\n"
    "s_add_u32 s13, s25, 81920\n"
    "v_add_u32 v160, s13, v160\n"
    "v_xor_b32 v161, 32, v160\n"
    "v_lshlrev_b32 v154, 6, v53\n"
    "v_lshl_or_b32 v154, v54, 8, v154\n"
    "v_and_b32 v54, 1, v49\n"
    "v_lshl_or_b32 v154, v54, 5, v154\n"
    "v_lshlrev_b32 v162, 3, v53\n"
    "v_lshrrev_b32 v53, 2, v48\n"
    "v_lshl_or_b32 v162, v53, 6, v162\n"
    "v_mul_u32_u24 v163, 272, v48\n"
    "v_lshl_add_u32 v163, v49, 3, v163\n"
    "v_add_u32 v163, s14, v163\n"
    "v_bfe_u32 v53, v47, 3, 2\n"
    "v_lshlrev_b32 v165, 10, v53\n"
    "v_lshrrev_b32 v54, 5, v47\n"
    "v_lshl_or_b32 v165, v54, 8, v165\n"
    "v_lshrrev_b32 v54, 1, v47\n"
    "v_xor_b32 v54, v54, v47\n"
    "v_lshrrev_b32 v54, 1, v54\n"
    "v_and_b32 v54, 4, v54\n"
    "v_xor_b32 v54, v54, v47\n"
    "v_and_b32 v54, 7, v54\n"
    "v_lshl_or_b32 v165, v54, 4, v165\n"
    "v_lshlrev_b32 v54, 2, v49\n"
    "v_sub_u32 v53, v48, v54\n"
    "v_add_u32 v53, s30, v53\n"
    "s_lshl_b32 s13, s30, 1\n"
    "v_add_u32 v55, 32, v53\n"
    "v_cmp_lt_u32_e64 s[52:53], s13, v55\n"
    "v_add_u32 v55, 31, v53\n"
    "v_cmp_lt_u32_e64 s[54:55], s13, v55\n"
    "v_add_u32 v55, 30, v53\n"
    "v_cmp_lt_u32_e64 s[56:57], s13, v55\n"
    "v_add_u32 v55, 29, v53\n"
    "v_cmp_lt_u32_e64 s[58:59], s13, v55\n"
    "v_add_u32 v55, 16, v53\n"
    "v_cmp_lt_u32_e64 s[60:61], s13, v55\n"
    "v_add_u32 v55, 15, v53\n"
    "v_cmp_lt_u32_e64 s[62:63], s13, v55\n"
    "v_add_u32 v55, 14, v53\n"
    "v_cmp_lt_u32_e64 s[64:65], s13, v55\n"
    "v_add_u32 v55, 13, v53\n"
    "v_cmp_lt_u32_e64 s[66:67], s13, v55\n"
    "v_add_u32 v55, 0, v53\n"
    "v_cmp_lt_u32_e64 s[68:69], s13, v55\n"
    "v_add_u32 v55, -1, v53\n"
    "v_cmp_lt_u32_e64 s[70:71], s13, v55\n"
    "v_add_u32 v55, -2, v53\n"
    "v_cmp_lt_u32_e64 s[72:73], s13, v55\n"
    "v_add_u32 v55, -3, v53\n"
    "v_cmp_lt_u32_e64 s[74:75], s13, v55\n"
    "v_add_u32 v55, -16, v53\n"
    "v_cmp_lt_u32_e64 s[76:77], s13, v55\n"
    "v_add_u32 v55, -17, v53\n"
    "v_cmp_lt_u32_e64 s[78:79], s13, v55\n"
    "v_add_u32 v55, -18, v53\n"
    "v_cmp_lt_u32_e64 s[80:81], s13, v55\n"
    "v_add_u32 v55, -19, v53\n"
    "v_cmp_lt_u32_e64 s[82:83], s13, v55\n"
    "v_add_u32 v55, -32, v53\n"
    "v_cmp_lt_u32_e64 s[84:85], s13, v55\n"
    "v_add_u32 v55, -33, v53\n"
    "v_cmp_lt_u32_e64 s[86:87], s13, v55\n"
    "v_add_u32 v55, -34, v53\n"
    "v_cmp_lt_u32_e64 s[88:89], s13, v55\n"
    "v_add_u32 v55, -35, v53\n"
    "v_cmp_lt_u32_e64 s[90:91], s13, v55\n"
    "v_add_u32 v55, 0, v54\n"
    "v_cmp_le_u32_e64 s[92:93], s31, v55\n"
    "v_add_u32 v55, 1, v54\n"
    "v_cmp_le_u32_e64 s[94:95], s31, v55\n"
    "v_add_u32 v55, 2, v54\n"
    "v_cmp_le_u32_e64 s[96:97], s31, v55\n"
    "v_add_u32 v55, 3, v54\n"
    "v_cmp_le_u32_e64 s[98:99], s31, v55\n"
    "v_mov_b32 v156, 0xff800000\n"
    "v_cndmask_b32_e64 v148, 0, v156, s[92:93]\n"
    "v_cndmask_b32_e64 v149, 0, v156, s[94:95]\n"
    "v_cndmask_b32_e64 v150, 0, v156, s[96:97]\n"
    "v_cndmask_b32_e64 v151, 0, v156, s[98:99]\n"
    "s_add_u32 s13, s26, -2\n"
    "s_cmp_ge_u32 s13, 128\n"
    "s_cselect_b64 s[18:19], -1, 0\n"
    "s_cmp_eq_u32 s13, 0\n"
    "s_cselect_b64 s[22:23], -1, 0\n"
    "s_andn2_b64 vcc, s[22:23], s[92:93]\n"
    "s_or_b64 vcc, s[52:53], vcc\n"
    "s_or_b64 vcc, vcc, s[18:19]\n"
    "v_cndmask_b32_e64 v144, 0, v156, vcc\n"
    "s_andn2_b64 vcc, s[22:23], s[94:95]\n"
    "s_or_b64 vcc, s[54:55], vcc\n"
    "s_or_b64 vcc, vcc, s[18:19]\n"
    "v_cndmask_b32_e64 v145, 0, v156, vcc\n"
    "s_andn2_b64 vcc, s[22:23], s[96:97]\n"
    "s_or_b64 vcc, s[56:57], vcc\n"
    "s_or_b64 vcc, vcc, s[18:19]\n"
    "v_cndmask_b32_e64 v146, 0, v156, vcc\n"
    "s_andn2_b64 vcc, s[22:23], s[98:99]\n"
    "s_or_b64 vcc, s[58:59], vcc\n"
    "s_or_b64 vcc, vcc, s[18:19]\n"
    "v_cndmask_b32_e64 v147, 0, v156, vcc\n"
    "s_add_u32 s13, s26, -1\n"
    "s_cmp_ge_u32 s13, 128\n"
    "s_cselect_b64 s[18:19], -1, 0\n"
    "s_cmp_eq_u32 s13, 0\n"
    "s_cselect_b64 s[22:23], -1, 0\n"
    "s_andn2_b64 vcc, s[22:23], s[92:93]\n"
    "s_or_b64 s[52:53], s[52:53], vcc\n"
    "s_or_b64 s[52:53], s[52:53], s[18:19]\n"
    "s_andn2_b64 vcc, s[22:23], s[94:95]\n"
    "s_or_b64 s[54:55], s[54:55], vcc\n"
    "s_or_b64 s[54:55], s[54:55], s[18:19]\n"
    "s_andn2_b64 vcc, s[22:23], s[96:97]\n"
    "s_or_b64 s[56:57], s[56:57], vcc\n"
    "s_or_b64 s[56:57], s[56:57], s[18:19]\n"
    "s_andn2_b64 vcc, s[22:23], s[98:99]\n"
    "s_or_b64 s[58:59], s[58:59], vcc\n"
    "s_or_b64 s[58:59], s[58:59], s[18:19]\n"
    "s_add_u32 s13, s26, -1\n"
    "s_cmp_ge_u32 s13, 128\n"
    "s_cselect_b64 s[18:19], -1, 0\n"
    "s_cmp_eq_u32 s13, 0\n"
    "s_cselect_b64 s[22:23], -1, 0\n"
    "s_andn2_b64 vcc, s[22:23], s[92:93]\n"
    "s_or_b64 vcc, s[60:61], vcc\n"
    "s_or_b64 vcc, vcc, s[18:19]\n"
    "v_cndmask_b32_e64 v128, 0, v156, vcc\n"
    "s_andn2_b64 vcc, s[22:23], s[94:95]\n"
    "s_or_b64 vcc, s[62:63], vcc\n"
    "s_or_b64 vcc, vcc, s[18:19]\n"
    "v_cndmask_b32_e64 v129, 0, v156, vcc\n"
    "s_andn2_b64 vcc, s[22:23], s[96:97]\n"
    "s_or_b64 vcc, s[64:65], vcc\n"
    "s_or_b64 vcc, vcc, s[18:19]\n"
    "v_cndmask_b32_e64 v130, 0, v156, vcc\n"
    "s_andn2_b64 vcc, s[22:23], s[98:99]\n"
    "s_or_b64 vcc, s[66:67], vcc\n"
    "s_or_b64 vcc, vcc, s[18:19]\n"
    "v_cndmask_b32_e64 v131, 0, v156, vcc\n"
    "s_add_u32 s13, s26, 0\n"
    "s_cmp_ge_u32 s13, 128\n"
    "s_cselect_b64 s[18:19], -1, 0\n"
    "s_cmp_eq_u32 s13, 0\n"
    "s_cselect_b64 s[22:23], -1, 0\n"
    "s_andn2_b64 vcc, s[22:23], s[92:93]\n"
    "s_or_b64 s[60:61], s[60:61], vcc\n"
    "s_or_b64 s[60:61], s[60:61], s[18:19]\n"
    "s_andn2_b64 vcc, s[22:23], s[94:95]\n"
    "s_or_b64 s[62:63], s[62:63], vcc\n"
    "s_or_b64 s[62:63], s[62:63], s[18:19]\n"
    "s_andn2_b64 vcc, s[22:23], s[96:97]\n"
    "s_or_b64 s[64:65], s[64:65], vcc\n"
    "s_or_b64 s[64:65], s[64:65], s[18:19]\n"
    "s_andn2_b64 vcc, s[22:23], s[98:99]\n"
    "s_or_b64 s[66:67], s[66:67], vcc\n"
    "s_or_b64 s[66:67], s[66:67], s[18:19]\n"
    "s_add_u32 s13, s26, 0\n"
    "s_cmp_eq_u32 s13, 0\n"
    "s_cselect_b64 s[22:23], -1, 0\n"
    "s_andn2_b64 vcc, s[22:23], s[92:93]\n"
    "s_or_b64 vcc, s[68:69], vcc\n"
    "v_cndmask_b32_e64 v132, 0, v156, vcc\n"
    "s_andn2_b64 vcc, s[22:23], s[94:95]\n"
    "s_or_b64 vcc, s[70:71], vcc\n"
    "v_cndmask_b32_e64 v133, 0, v156, vcc\n"
    "s_andn2_b64 vcc, s[22:23], s[96:97]\n"
    "s_or_b64 vcc, s[72:73], vcc\n"
    "v_cndmask_b32_e64 v134, 0, v156, vcc\n"
    "s_andn2_b64 vcc, s[22:23], s[98:99]\n"
    "s_or_b64 vcc, s[74:75], vcc\n"
    "v_cndmask_b32_e64 v135, 0, v156, vcc\n"
    "s_add_u32 s13, s26, 1\n"
    "s_cmp_ge_u32 s13, 128\n"
    "s_cselect_b64 s[18:19], -1, 0\n"
    "s_or_b64 vcc, s[76:77], s[18:19]\n"
    "v_cndmask_b32_e64 v136, 0, v156, vcc\n"
    "s_or_b64 vcc, s[78:79], s[18:19]\n"
    "v_cndmask_b32_e64 v137, 0, v156, vcc\n"
    "s_or_b64 vcc, s[80:81], s[18:19]\n"
    "v_cndmask_b32_e64 v138, 0, v156, vcc\n"
    "s_or_b64 vcc, s[82:83], s[18:19]\n"
    "v_cndmask_b32_e64 v139, 0, v156, vcc\n"
    "s_add_u32 s13, s26, 2\n"
    "s_cmp_ge_u32 s13, 128\n"
    "s_cselect_b64 s[18:19], -1, 0\n"
    "s_or_b64 s[76:77], s[76:77], s[18:19]\n"
    "s_or_b64 s[78:79], s[78:79], s[18:19]\n"
    "s_or_b64 s[80:81], s[80:81], s[18:19]\n"
    "s_or_b64 s[82:83], s[82:83], s[18:19]\n"
    "s_add_u32 s13, s26, 2\n"
    "s_cmp_ge_u32 s13, 128\n"
    "s_cselect_b64 s[18:19], -1, 0\n"
    "s_or_b64 vcc, s[84:85], s[18:19]\n"
    "v_cndmask_b32_e64 v140, 0, v156, vcc\n"
    "s_or_b64 vcc, s[86:87], s[18:19]\n"
    "v_cndmask_b32_e64 v141, 0, v156, vcc\n"
    "s_or_b64 vcc, s[88:89], s[18:19]\n"
    "v_cndmask_b32_e64 v142, 0, v156, vcc\n"
    "s_or_b64 vcc, s[90:91], s[18:19]\n"
    "v_cndmask_b32_e64 v143, 0, v156, vcc\n"
    "s_add_u32 s13, s26, 3\n"
    "s_cmp_ge_u32 s13, 128\n"
    "s_cselect_b64 s[18:19], -1, 0\n"
    "s_or_b64 s[84:85], s[84:85], s[18:19]\n"
    "s_or_b64 s[86:87], s[86:87], s[18:19]\n"
    "s_or_b64 s[88:89], s[88:89], s[18:19]\n"
    "s_or_b64 s[90:91], s[90:91], s[18:19]\n"
    "s_cmp_eq_u32 s15, 0\n"
    "s_cbranch_scc0 L_q0%=\n"
    "s_add_u32 m0, s16, 0\n"
    "v_add_u32 v159, 0, v157\n"
    "buffer_load_dwordx4 v159, s[44:47], 0 offen nt lds\n"
    "s_add_u32 m0, s16, 1024\n"
    "v_add_u32 v159, 128, v157\n"
    "buffer_load_dwordx4 v159, s[44:47], 0 offen nt lds\n"
    "s_add_u32 m0, s16, 2048\n"
    "v_add_u32 v159, 0, v158\n"
    "buffer_load_dwordx4 v159, s[44:47], 0 offen nt lds\n"
    "s_add_u32 m0, s16, 3072\n"
    "v_add_u32 v159, 128, v158\n"
    "buffer_load_dwordx4 v159, s[44:47], 0 offen nt lds\n"
    "s_add_u32 m0, s16, 4096\n"
    "v_add_u32 v159, 4096, v157\n"
    "buffer_load_dwordx4 v159, s[44:47], 0 offen nt lds\n"
    "s_add_u32 m0, s16, 5120\n"
    "v_add_u32 v159, 4224, v157\n"
    "buffer_load_dwordx4 v159, s[44:47], 0 offen nt lds\n"
    "s_add_u32 m0, s16, 6144\n"
    "v_add_u32 v159, 4096, v158\n"
    "buffer_load_dwordx4 v159, s[44:47], 0 offen nt lds\n"
    "s_add_u32 m0, s16, 7168\n"
    "v_add_u32 v159, 4224, v158\n"
    "buffer_load_dwordx4 v159, s[44:47], 0 offen nt lds\n"
    "L_q0%=:\n"
    "s_setprio 0\n"
    "s_barrier\n"
    "s_cmp_eq_u32 s15, 0\n"
    "s_cbranch_scc1 L_w1%=\n"
    "s_waitcnt vmcnt(0)\n"
    "L_w1%=:\n"
    "s_lshr_b32 s13, %[bid], 3\n"
    "s_lshl_b32 s13, s13, 4\n"
    "s_or_b32 s13, s13, s33\n"
    "s_cmp_eq_u32 s13, 116\n"
    "s_cbranch_scc0 L_noorg%=\n"
    "s_and_b32 s13, %[bid], 7\n"
    "s_lshl_b32 s13, s13, 2\n"
    "v_lshl_add_u32 v53, v47, 7, s13\n"
    "v_mov_b32 v54, s8\n"
    "s_setprio 3\n"
    "s_mov_b64 exec, 0xff\n"
    "buffer_store_dword v54, v53, s[36:39], 0 offen sc1\n"
    "s_mov_b64 exec, -1\n"
    "s_setprio 0\n"
    "L_noorg%=:\n"
    "s_waitcnt vmcnt(8)\n"
    "s_barrier\n"
    "ds_read_b128 v[16:19], v152 offset:0\n"
    "ds_read_b128 v[20:23], v153 offset:0\n"
    "ds_read_b128 v[24:27], v152 offset:1024\n"
    "ds_read_b128 v[28:31], v153 offset:1024\n"
    "ds_read_b128 v[32:35], v152 offset:4096\n"
    "ds_read_b128 v[36:39], v153 offset:4096\n"
    "ds_read_b128 v[40:43], v152 offset:5120\n"
    "ds_read_b128 v[44:47], v153 offset:5120\n"
    "ds_read_b128 v[48:51], v152 offset:8192\n"
    "ds_read_b128 v[52:55], v153 offset:8192\n"
    "ds_read_b128 v[56:59], v152 offset:9216\n"
    "ds_read_b128 v[60:63], v153 offset:9216\n"
    "ds_read_b128 v[64:67], v152 offset:12288\n"
    "ds_read_b128 v[68:71], v153 offset:12288\n"
    "ds_read_b128 v[72:75], v152 offset:13312\n"
    "ds_read_b128 v[76:79], v153 offset:13312\n"
    "ds_read_b128 v[80:83], v152 offset:16384\n"
    "ds_read_b128 v[84:87], v153 offset:16384\n"
    "ds_read_b128 v[88:91], v152 offset:17408\n"
    "ds_read_b128 v[92:95], v153 offset:17408\n"
    "ds_read_b128 v[0:3], v154 offset:0\n"
    "ds_read_b128 v[4:7], v154 offset:16\n"
    "ds_read_b128 v[8:11], v154 offset:512\n"
    "ds_read_b128 v[12:15], v154 offset:528\n"
    "s_mul_i32 s13, s20, 45056\n"
    "s_add_u32 s13, s13, s21\n"
    "s_cmp_eq_u32 s12, 7\n"
    "s_cselect_b32 s13, s13, 0\n"
    "v_add_u32 v152, s13, v152\n"
    "v_add_u32 v153, s13, v153\n"
    "ds_read_b128 v[96:99], v152 offset:20480\n"
    "ds_read_b128 v[100:103], v153 offset:20480\n"
    "ds_read_b128 v[104:107], v152 offset:21504\n"
    "ds_read_b128 v[108:111], v153 offset:21504\n"
    "s_cmp_eq_u32 s15, 0\n"
    "s_cbranch_scc1 L_q1%=\n"
    "s_waitcnt lgkmcnt(0)\n"
    "s_barrier\n"
    "s_setprio 2\n"
    "s_mul_i32 s13, s20, 65536\n"
    "s_add_u32 m0, s17, s13\n"
    "v_add_u32 v159, s13, v155\n"
    "buffer_load_dwordx4 v159, s[40:43], 0 offen nt lds\n"
    "s_mul_i32 s13, s20, 77824\n"
    "s_add_u32 m0, s17, s13\n"
    "v_add_u32 v159, s13, v155\n"
    "buffer_load_dwordx4 v159, s[40:43], 0 offen nt lds\n"
    "s_mul_i32 s13, s20, 49152\n"
    "s_add_u32 m0, s17, s13\n"
    "v_add_u32 v159, s13, v155\n"
    "buffer_load_dwordx4 v159, s[40:43], 0 offen nt lds\n"
    "s_mul_i32 s13, s20, 53248\n"
    "s_add_u32 m0, s17, s13\n"
    "v_add_u32 v159, s13, v155\n"
    "buffer_load_dwordx4 v159, s[40:43], 0 offen nt lds\n"
    "s_mul_i32 s13, s20, 69632\n"
    "s_add_u32 m0, s17, s13\n"
    "v_add_u32 v159, s13, v155\n"
    "buffer_load_dwordx4 v159, s[40:43], 0 offen nt lds\n"
    "s_mul_i32 s13, s20, 73728\n"
    "s_add_u32 m0, s17, s13\n"
    "v_add_u32 v159, s13, v155\n"
    "buffer_load_dwordx4 v159, s[40:43], 0 offen nt lds\n"
    "s_mul_i32 s13, s20, 57344\n"
    "s_add_u32 m0, s17, s13\n"
    "v_add_u32 v159, s13, v155\n"
    "buffer_load_dwordx4 v159, s[40:43], 0 offen nt lds\n"
    "s_mul_i32 s13, s20, 61440\n"
    "s_add_u32 m0, s17, s13\n"
    "v_add_u32 v159, s13, v155\n"
    "buffer_load_dwordx4 v159, s[40:43], 0 offen nt lds\n"
    "s_add_u32 m0, s16, 0\n"
    "v_add_u32 v159, 0, v157\n"
    "buffer_load_dwordx4 v159, s[44:47], 0 offen nt lds\n"
    "s_add_u32 m0, s16, 1024\n"
    "v_add_u32 v159, 128, v157\n"
    "buffer_load_dwordx4 v159, s[44:47], 0 offen nt lds\n"
    "s_add_u32 m0, s16, 2048\n"
    "v_add_u32 v159, 0, v158\n"
    "buffer_load_dwordx4 v159, s[44:47], 0 offen nt lds\n"
    "s_add_u32 m0, s16, 3072\n"
    "v_add_u32 v159, 128, v158\n"
    "buffer_load_dwordx4 v159, s[44:47], 0 offen nt lds\n"
    "s_add_u32 m0, s16, 4096\n"
    "v_add_u32 v159, 4096, v157\n"
    "buffer_load_dwordx4 v159, s[44:47], 0 offen nt lds\n"
    "s_add_u32 m0, s16, 5120\n"
    "v_add_u32 v159, 4224, v157\n"
    "buffer_load_dwordx4 v159, s[44:47], 0 offen nt lds\n"
    "s_add_u32 m0, s16, 6144\n"
    "v_add_u32 v159, 4096, v158\n"
    "buffer_load_dwordx4 v159, s[44:47], 0 offen nt lds\n"
    "s_add_u32 m0, s16, 7168\n"
    "v_add_u32 v159, 4224, v158\n"
    "buffer_load_dwordx4 v159, s[44:47], 0 offen nt lds\n"
    "s_setprio 1\n"
    "L_q1%=:\n"
    "v_xor_b32 v159, 16, v164\n"
    "v_mov_b32 v152, 0x3f803f80\n"
    "v_mov_b32 v153, 0x3f803f80\n"
    "v_mov_b32 v154, 0x3f803f80\n"
    "v_mov_b32 v155, 0x3f803f80\n"
    "s_cmp_eq_u32 s15, 0\n"
    "s_cbranch_scc1 L_q2%=\n"
    "v_mov_b32 v156, 0\n"
    "v_mov_b32 v157, 1\n"
    "s_mov_b64 exec, 1\n"
    "s_waitcnt vmcnt(8)\n"
    "ds_add_u32 v156, v157 offset:3076\n"
    "s_mov_b64 exec, -1\n"
    "L_q2%=:\n"
    "s_waitcnt vmcnt(4) lgkmcnt(0)\n"
    "ds_read_b128 v[112:115], v164 offset:0\n"
    "ds_read_b128 v[116:119], v159 offset:0\n"
    "ds_read_b128 v[120:123], v164 offset:1024\n"
    "ds_read_b128 v[124:127], v159 offset:1024\n"
    "s_waitcnt lgkmcnt(3)\n"
    "v_mfma_f32_16x16x32_bf16 v[144:147], v[16:19], v[112:115], v[144:147]\n"
    "s_waitcnt lgkmcnt(2)\n"
    "v_mfma_f32_16x16x32_bf16 v[144:147], v[20:23], v[116:119], v[144:147]\n"
    "s_waitcnt lgkmcnt(1)\n"
    "v_mfma_f32_16x16x32_bf16 v[144:147], v[24:27], v[120:123], v[144:147]\n"
    "s_waitcnt lgkmcnt(0)\n"
    "v_mfma_f32_16x16x32_bf16 v[144:147], v[28:31], v[124:127], v[144:147]\n"
    "v_mfma_f32_16x16x32_bf16 v[128:131], v[32:35], v[112:115], v[128:131]\n"
    "v_mfma_f32_16x16x32_bf16 v[128:131], v[36:39], v[116:119], v[128:131]\n"
    "v_mfma_f32_16x16x32_bf16 v[128:131], v[40:43], v[120:123], v[128:131]\n"
    "v_mfma_f32_16x16x32_bf16 v[128:131], v[44:47], v[124:127], v[128:131]\n"
    "v_mfma_f32_16x16x32_bf16 v[132:135], v[48:51], v[112:115], v[132:135]\n"
    "v_mfma_f32_16x16x32_bf16 v[132:135], v[52:55], v[116:119], v[132:135]\n"
    "v_mfma_f32_16x16x32_bf16 v[132:135], v[56:59], v[120:123], v[132:135]\n"
    "v_mfma_f32_16x16x32_bf16 v[132:135], v[60:63], v[124:127], v[132:135]\n"
    "v_max_f32 v156, v144, v145\n"
    "v_mfma_f32_16x16x32_bf16 v[136:139], v[64:67], v[112:115], v[136:139]\n"
    "v_max_f32 v157, v146, v147\n"
    "v_mfma_f32_16x16x32_bf16 v[136:139], v[68:71], v[116:119], v[136:139]\n"
    "v_mfma_f32_16x16x32_bf16 v[136:139], v[72:75], v[120:123], v[136:139]\n"
    "v_mfma_f32_16x16x32_bf16 v[136:139], v[76:79], v[124:127], v[136:139]\n"
    "v_max3_f32 v156, v156, v128, v129\n"
    "v_mfma_f32_16x16x32_bf16 v[140:143], v[80:83], v[112:115], v[140:143]\n"
    "v_max3_f32 v157, v157, v130, v131\n"
    "v_mfma_f32_16x16x32_bf16 v[140:143], v[84:87], v[116:119], v[140:143]\n"
    "v_mfma_f32_16x16x32_bf16 v[140:143], v[88:91], v[120:123], v[140:143]\n"
    "v_mfma_f32_16x16x32_bf16 v[140:143], v[92:95], v[124:127], v[140:143]\n"
    "v_max3_f32 v156, v156, v132, v133\n"
    "v_mfma_f32_16x16x32_bf16 v[148:151], v[0:3], v[112:115], v[148:151]\n"
    "v_max3_f32 v157, v157, v134, v135\n"
    "v_mfma_f32_16x16x32_bf16 v[148:151], v[4:7], v[116:119], v[148:151]\n"
    "v_mfma_f32_16x16x32_bf16 v[148:151], v[8:11], v[120:123], v[148:151]\n"
    "v_mfma_f32_16x16x32_bf16 v[148:151], v[12:15], v[124:127], v[148:151]\n"
    "s_cmp_eq_u32 s15, 0\n"
    "s_cbranch_scc0 L_qb0%=\n"
    "s_waitcnt vmcnt(0)\n"
    "ds_read_b128 v[16:19], v164 offset:4096\n"
    "ds_read_b128 v[20:23], v159 offset:4096\n"
    "ds_read_b128 v[24:27], v164 offset:5120\n"
    "ds_read_b128 v[28:31], v159 offset:5120\n"
    "s_waitcnt lgkmcnt(0)\n"
    "s_barrier\n"
    "v_readlane_b32 s40, v167, 0\n"
    "v_readlane_b32 s41, v167, 1\n"
    "v_readlane_b32 s42, v167, 2\n"
    "v_readlane_b32 s43, v167, 3\n"
    "v_readlane_b32 s16, v167, 4\n"
    "v_readlane_b32 s17, v167, 5\n"
    "v_readlane_b32 s20, v167, 6\n"
    "v_readlane_b32 s21, v167, 7\n"
    "v_mbcnt_lo_u32_b32 v112, -1, 0\n"
    "v_mbcnt_hi_u32_b32 v112, -1, v112\n"
    "s_and_b32 s13, %[bid], 7\n"
    "s_lshl_b32 s13, s13, 7\n"
    "v_and_b32 v112, 7, v112\n"
    "v_lshl_add_u32 v112, v112, 2, s13\n"
    "s_setprio 3\n"
    "buffer_load_dword v167, v112, s[36:39], 0 offen sc1\n"
    "s_setprio 0\n"
    "L_qb0%=:\n"
    "v_mov_b32 v125, 0\n"
    "v_mov_b32 v126, 1\n"
    "v_mov_b32 v127, 8\n"
    "v_mov_b32 v124, 0xff800000\n"
    "v_max3_f32 v156, v156, v136, v137\n"
    "v_max3_f32 v157, v157, v138, v139\n"
    "v_max3_f32 v156, v156, v140, v141\n"
    "v_max3_f32 v157, v157, v142, v143\n"
    "v_max3_f32 v156, v156, v148, v149\n"
    "v_max3_f32 v157, v157, v150, v151\n"
    "v_max_f32 v156, v156, v157\n"
    "v_mov_b32 v157, v156\n"
    "s_nop 1\n"
    "v_permlane16_swap_b32 v156, v157\n"
    "v_max_f32 v156, v156, v157\n"
    "v_mov_b32 v157, v156\n"
    "s_nop 1\n"
    "v_permlane32_swap_b32 v156, v157\n"
    "v_max_f32 v156, v156, v157\n"
    "v_mul_f32 v158, 0xbe0293ee, v156\n"
    "v_fmamk_f32 v128, v128, 0x3e0293ee, v158\n"
    "v_fmamk_f32 v129, v129, 0x3e0293ee, v158\n"
    "v_fmamk_f32 v130, v130, 0x3e0293ee, v158\n"
    "v_fmamk_f32 v131, v131, 0x3e0293ee, v158\n"
    "v_fmamk_f32 v132, v132, 0x3e0293ee, v158\n"
    "v_fmamk_f32 v133, v133, 0x3e0293ee, v158\n"
    "v_fmamk_f32 v134, v134, 0x3e0293ee, v158\n"
    "v_fmamk_f32 v135, v135, 0x3e0293ee, v158\n"
    "v_fmamk_f32 v136, v136, 0x3e0293ee, v158\n"
    "v_fmamk_f32 v137, v137, 0x3e0293ee, v158\n"
    "v_fmamk_f32 v138, v138, 0x3e0293ee, v158\n"
    "v_fmamk_f32 v139, v139, 0x3e0293ee, v158\n"
    "v_fmamk_f32 v140, v140, 0x3e0293ee, v158\n"
    "v_fmamk_f32 v141, v141, 0x3e0293ee, v158\n"
    "v_fmamk_f32 v142, v142, 0x3e0293ee, v158\n"
    "v_fmamk_f32 v143, v143, 0x3e0293ee, v158\n"
    "v_fmamk_f32 v144, v144, 0x3e0293ee, v158\n"
    "v_fmamk_f32 v145, v145, 0x3e0293ee, v158\n"
    "v_fmamk_f32 v146, v146, 0x3e0293ee, v158\n"
    "v_fmamk_f32 v147, v147, 0x3e0293ee, v158\n"
    "v_fmamk_f32 v148, v148, 0x3e0293ee, v158\n"
    "v_fmamk_f32 v149, v149, 0x3e0293ee, v158\n"
    "v_fmamk_f32 v150, v150, 0x3e0293ee, v158\n"
    "v_fmamk_f32 v151, v151, 0x3e0293ee, v158\n"
    "v_exp_f32 v128, v128\n"
    "v_exp_f32 v129, v129\n"
    "v_exp_f32 v130, v130\n"
    "v_cvt_pk_bf16_f32 v112, v128, v129\n"
    "v_exp_f32 v131, v131\n"
    "v_exp_f32 v132, v132\n"
    "v_cvt_pk_bf16_f32 v113, v130, v131\n"
    "v_exp_f32 v133, v133\n"
    "v_exp_f32 v134, v134\n"
    "v_cvt_pk_bf16_f32 v114, v132, v133\n"
    "v_exp_f32 v135, v135\n"
    "v_exp_f32 v136, v136\n"
    "v_cvt_pk_bf16_f32 v115, v134, v135\n"
    "v_exp_f32 v137, v137\n"
    "v_exp_f32 v138, v138\n"
    "v_cvt_pk_bf16_f32 v116, v136, v137\n"
    "v_exp_f32 v139, v139\n"
    "v_exp_f32 v140, v140\n"
    "v_cvt_pk_bf16_f32 v117, v138, v139\n"
    "v_exp_f32 v141, v141\n"
    "v_exp_f32 v142, v142\n"
    "v_cvt_pk_bf16_f32 v118, v140, v141\n"
    "v_exp_f32 v143, v143\n"
    "v_exp_f32 v144, v144\n"
    "v_cvt_pk_bf16_f32 v119, v142, v143\n"
    "v_exp_f32 v145, v145\n"
    "v_exp_f32 v146, v146\n"
    "v_cvt_pk_bf16_f32 v120, v144, v145\n"
    "v_exp_f32 v147, v147\n"
    "v_exp_f32 v148, v148\n"
    "v_cvt_pk_bf16_f32 v121, v146, v147\n"
    "v_exp_f32 v149, v149\n"
    "v_exp_f32 v150, v150\n"
    "v_cvt_pk_bf16_f32 v122, v148, v149\n"
    "v_exp_f32 v151, v151\n"
    "s_nop 0\n"
    "v_cvt_pk_bf16_f32 v123, v150, v151\n"
    "s_cmp_eq_u32 s15, 0\n"
    "s_cbranch_scc1 L_qb1%=\n"
    "s_waitcnt vmcnt(0)\n"
    "ds_read_b128 v[16:19], v164 offset:4096\n"
    "ds_read_b128 v[20:23], v159 offset:4096\n"
    "ds_read_b128 v[24:27], v164 offset:5120\n"
    "ds_read_b128 v[28:31], v159 offset:5120\n"
    "ds_read_b32 v127, v125 offset:3076\n"
    "s_mov_b64 exec, 1\n"
    "ds_add_u32 v125, v126 offset:3080\n"
    "s_mov_b64 exec, -1\n"
    "L_qb1%=:\n"
    "v_mfma_f32_16x16x32_bf16 v[156:159], v[152:155], v[112:115], 0\n"
    "v_cndmask_b32_e64 v148, 0, v124, s[84:85]\n"
    "v_cndmask_b32_e64 v149, 0, v124, s[86:87]\n"
    "v_mfma_f32_16x16x32_bf16 v[156:159], v[152:155], v[116:119], v[156:159]\n"
    "v_cndmask_b32_e64 v150, 0, v124, s[88:89]\n"
    "v_cndmask_b32_e64 v151, 0, v124, s[90:91]\n"
    "v_mfma_f32_16x16x32_bf16 v[156:159], v[152:155], v[120:123], v[156:159]\n"
    "s_waitcnt lgkmcnt(0)\n"
    "v_mfma_f32_16x16x32_bf16 v[148:151], v[96:99], v[16:19], v[148:151]\n"
    "v_cndmask_b32_e64 v140, 0, v124, s[76:77]\n"
    "v_cndmask_b32_e64 v141, 0, v124, s[78:79]\n"
    "v_mfma_f32_16x16x32_bf16 v[148:151], v[100:103], v[20:23], v[148:151]\n"
    "v_cndmask_b32_e64 v142, 0, v124, s[80:81]\n"
    "v_cndmask_b32_e64 v143, 0, v124, s[82:83]\n"
    "v_mfma_f32_16x16x32_bf16 v[148:151], v[104:107], v[24:27], v[148:151]\n"
    "v_mfma_f32_16x16x32_bf16 v[148:151], v[108:111], v[28:31], v[148:151]\n"
    "v_rcp_f32 v166, v156\n"
    "L_f4y_w%=:\n"
    "s_waitcnt lgkmcnt(0)\n"
    "v_readfirstlane_b32 s13, v127\n"
    "s_nop 1\n"
    "s_cmp_ge_u32 s13, 8\n"
    "s_cbranch_scc1 L_f4y_d%=\n"
    "s_sleep 1\n"
    "ds_read_b32 v127, v125 offset:3076\n"
    "s_branch L_f4y_w%=\n"
    "L_f4y_d%=:\n"
    "v_mfma_f32_16x16x32_bf16 v[140:143], v[80:83], v[16:19], v[140:143]\n"
    "ds_read_b64_tr_b16 v[98:99], v160 offset:4096\n"
    "v_cndmask_b32_e64 v136, 0, v124, s[68:69]\n"
    "v_mfma_f32_16x16x32_bf16 v[140:143], v[84:87], v[20:23], v[140:143]\n"
    "ds_read_b64_tr_b16 v[100:101], v160 offset:8192\n"
    "v_cndmask_b32_e64 v137, 0, v124, s[70:71]\n"
    "v_cndmask_b32_e64 v138, 0, v124, s[72:73]\n"
    "v_mfma_f32_16x16x32_bf16 v[140:143], v[88:91], v[24:27], v[140:143]\n"
    "ds_read_b64_tr_b16 v[102:103], v160 offset:12288\n"
    "v_cndmask_b32_e64 v139, 0, v124, s[74:75]\n"
    "v_mfma_f32_16x16x32_bf16 v[140:143], v[92:95], v[28:31], v[140:143]\n"
    "ds_read_b64_tr_b16 v[104:105], v160 offset:16384\n"
    "v_max_f32 v156, v148, v149\n"
    "v_mfma_f32_16x16x32_bf16 v[136:139], v[64:67], v[16:19], v[136:139]\n"
    "ds_read_b64_tr_b16 v[106:107], v160 offset:0\n"
    "v_cndmask_b32_e64 v132, 0, v124, s[60:61]\n"
    "v_max_f32 v157, v150, v151\n"
    "v_mfma_f32_16x16x32_bf16 v[136:139], v[68:71], v[20:23], v[136:139]\n"
    "ds_read_b64_tr_b16 v[108:109], v162 offset:1024\n"
    "v_cndmask_b32_e64 v133, 0, v124, s[62:63]\n"
    "v_cndmask_b32_e64 v134, 0, v124, s[64:65]\n"
    "v_mfma_f32_16x16x32_bf16 v[136:139], v[72:75], v[24:27], v[136:139]\n"
    "ds_read_b64_tr_b16 v[84:85], v161 offset:4096\n"
    "v_cndmask_b32_e64 v135, 0, v124, s[66:67]\n"
    "v_mfma_f32_16x16x32_bf16 v[136:139], v[76:79], v[28:31], v[136:139]\n"
    "ds_read_b64_tr_b16 v[86:87], v161 offset:8192\n"
    "v_max3_f32 v156, v156, v140, v141\n"
    "v_mfma_f32_16x16x32_bf16 v[132:135], v[48:51], v[16:19], v[132:135]\n"
    "ds_read_b64_tr_b16 v[88:89], v161 offset:12288\n"
    "v_cndmask_b32_e64 v128, 0, v124, s[52:53]\n"
    "v_max3_f32 v157, v157, v142, v143\n"
    "v_mfma_f32_16x16x32_bf16 v[132:135], v[52:55], v[20:23], v[132:135]\n"
    "ds_read_b64_tr_b16 v[90:91], v161 offset:16384\n"
    "v_cndmask_b32_e64 v129, 0, v124, s[54:55]\n"
    "v_cndmask_b32_e64 v130, 0, v124, s[56:57]\n"
    "v_mfma_f32_16x16x32_bf16 v[132:135], v[56:59], v[24:27], v[132:135]\n"
    "ds_read_b64_tr_b16 v[92:93], v161 offset:0\n"
    "v_cndmask_b32_e64 v131, 0, v124, s[58:59]\n"
    "v_mfma_f32_16x16x32_bf16 v[132:135], v[60:63], v[28:31], v[132:135]\n"
    "ds_read_b64_tr_b16 v[94:95], v162 offset:1056\n"
    "v_max3_f32 v156, v156, v136, v137\n"
    "v_mfma_f32_16x16x32_bf16 v[128:131], v[32:35], v[16:19], v[128:131]\n"
    "ds_read_b64_tr_b16 v[70:71], v160 offset:4608\n"
    "v_cndmask_b32_e64 v144, 0, v124, s[92:93]\n"
    "v_max3_f32 v157, v157, v138, v139\n"
    "v_mfma_f32_16x16x32_bf16 v[128:131], v[36:39], v[20:23], v[128:131]\n"
    "ds_read_b64_tr_b16 v[72:73], v160 offset:8704\n"
    "v_cndmask_b32_e64 v145, 0, v124, s[94:95]\n"
    "v_cndmask_b32_e64 v146, 0, v124, s[96:97]\n"
    "v_mfma_f32_16x16x32_bf16 v[128:131], v[40:43], v[24:27], v[128:131]\n"
    "ds_read_b64_tr_b16 v[74:75], v160 offset:12800\n"
    "v_cndmask_b32_e64 v147, 0, v124, s[98:99]\n"
    "v_mfma_f32_16x16x32_bf16 v[128:131], v[44:47], v[28:31], v[128:131]\n"
    "ds_read_b64_tr_b16 v[76:77], v160 offset:16896\n"
    "v_max3_f32 v156, v156, v132, v133\n"
    "v_mfma_f32_16x16x32_bf16 v[144:147], v[0:3], v[16:19], v[144:147]\n"
    "ds_read_b64_tr_b16 v[78:79], v160 offset:512\n"
    "v_max3_f32 v157, v157, v134, v135\n"
    "v_mfma_f32_16x16x32_bf16 v[144:147], v[4:7], v[20:23], v[144:147]\n"
    "ds_read_b64_tr_b16 v[80:81], v162 offset:1280\n"
    "v_lshrrev_b32 v164, 8, v165\n"
    "v_mfma_f32_16x16x32_bf16 v[144:147], v[8:11], v[24:27], v[144:147]\n"
    "ds_read_b64_tr_b16 v[56:57], v161 offset:4608\n"
    "v_lshl_add_u32 v164, v164, 4, v165\n"
    "v_mfma_f32_16x16x32_bf16 v[144:147], v[12:15], v[28:31], v[144:147]\n"
    "ds_read_b64_tr_b16 v[58:59], v161 offset:8704\n"
    "ds_read_b64_tr_b16 v[60:61], v161 offset:12800\n"
    "ds_read_b64_tr_b16 v[62:63], v161 offset:16896\n"
    "ds_read_b64_tr_b16 v[64:65], v161 offset:512\n"
    "ds_read_b64_tr_b16 v[66:67], v162 offset:1312\n"
    "v_add_u32 v164, s14, v164\n"
    "v_max3_f32 v156, v156, v128, v129\n"
    "v_max3_f32 v157, v157, v130, v131\n"
    "v_max3_f32 v156, v156, v144, v145\n"
    "v_max3_f32 v157, v157, v146, v147\n"
    "v_max_f32 v156, v156, v157\n"
    "v_mov_b32 v157, v156\n"
    "s_nop 1\n"
    "v_permlane16_swap_b32 v156, v157\n"
    "v_max_f32 v156, v156, v157\n"
    "v_mov_b32 v157, v156\n"
    "s_nop 1\n"
    "v_permlane32_swap_b32 v156, v157\n"
    "v_max_f32 v156, v156, v157\n"
    "v_mul_f32 v158, 0xbe0293ee, v156\n"
    "v_fmamk_f32 v128, v128, 0x3e0293ee, v158\n"
    "v_fmamk_f32 v129, v129, 0x3e0293ee, v158\n"
    "v_fmamk_f32 v130, v130, 0x3e0293ee, v158\n"
    "v_fmamk_f32 v131, v131, 0x3e0293ee, v158\n"
    "v_fmamk_f32 v132, v132, 0x3e0293ee, v158\n"
    "v_fmamk_f32 v133, v133, 0x3e0293ee, v158\n"
    "v_fmamk_f32 v134, v134, 0x3e0293ee, v158\n"
    "v_fmamk_f32 v135, v135, 0x3e0293ee, v158\n"
    "v_fmamk_f32 v136, v136, 0x3e0293ee, v158\n"
    "v_fmamk_f32 v137, v137, 0x3e0293ee, v158\n"
    "v_fmamk_f32 v138, v138, 0x3e0293ee, v158\n"
    "v_fmamk_f32 v139, v139, 0x3e0293ee, v158\n"
    "v_fmamk_f32 v140, v140, 0x3e0293ee, v158\n"
    "v_fmamk_f32 v141, v141, 0x3e0293ee, v158\n"
    "v_fmamk_f32 v142, v142, 0x3e0293ee, v158\n"
    "v_fmamk_f32 v143, v143, 0x3e0293ee, v158\n"
    "v_fmamk_f32 v144, v144, 0x3e0293ee, v158\n"
    "v_fmamk_f32 v145, v145, 0x3e0293ee, v158\n"
    "v_fmamk_f32 v146, v146, 0x3e0293ee, v158\n"
    "v_fmamk_f32 v147, v147, 0x3e0293ee, v158\n"
    "v_fmamk_f32 v148, v148, 0x3e0293ee, v158\n"
    "v_fmamk_f32 v149, v149, 0x3e0293ee, v158\n"
    "v_fmamk_f32 v150, v150, 0x3e0293ee, v158\n"
    "v_fmamk_f32 v151, v151, 0x3e0293ee, v158\n"
    "v_exp_f32 v128, v128\n"
    "v_exp_f32 v129, v129\n"
    "v_exp_f32 v130, v130\n"
    "v_cvt_pk_bf16_f32 v128, v128, v129\n"
    "v_exp_f32 v131, v131\n"
    "v_exp_f32 v132, v132\n"
    "v_cvt_pk_bf16_f32 v129, v130, v131\n"
    "v_exp_f32 v133, v133\n"
    "v_exp_f32 v134, v134\n"
    "v_cvt_pk_bf16_f32 v130, v132, v133\n"
    "v_exp_f32 v135, v135\n"
    "v_exp_f32 v136, v136\n"
    "v_cvt_pk_bf16_f32 v131, v134, v135\n"
    "v_exp_f32 v137, v137\n"
    "v_exp_f32 v138, v138\n"
    "v_cvt_pk_bf16_f32 v132, v136, v137\n"
    "v_exp_f32 v139, v139\n"
    "v_exp_f32 v140, v140\n"
    "v_cvt_pk_bf16_f32 v133, v138, v139\n"
    "v_exp_f32 v141, v141\n"
    "v_exp_f32 v142, v142\n"
    "v_cvt_pk_bf16_f32 v134, v140, v141\n"
    "v_exp_f32 v143, v143\n"
    "v_exp_f32 v144, v144\n"
    "v_cvt_pk_bf16_f32 v135, v142, v143\n"
    "v_exp_f32 v145, v145\n"
    "v_exp_f32 v146, v146\n"
    "v_cvt_pk_bf16_f32 v136, v144, v145\n"
    "v_exp_f32 v147, v147\n"
    "v_exp_f32 v148, v148\n"
    "v_cvt_pk_bf16_f32 v137, v146, v147\n"
    "v_exp_f32 v149, v149\n"
    "v_exp_f32 v150, v150\n"
    "v_cvt_pk_bf16_f32 v138, v148, v149\n"
    "v_exp_f32 v151, v151\n"
    "s_nop 0\n"
    "v_cvt_pk_bf16_f32 v139, v150, v151\n"
    "s_cmp_eq_u32 s15, 0\n"
    "s_cbranch_scc0 L_b3%=\n"
    "v_mov_b32 v125, 0\n"
    "L_f3o_r%=:\n"
    "ds_read_b32 v126, v125 offset:3072\n"
    "L_f3o_w%=:\n"
    "s_waitcnt lgkmcnt(0)\n"
    "v_readfirstlane_b32 s13, v126\n"
    "s_nop 1\n"
    "s_cmp_ge_u32 s13, 4\n"
    "s_cbranch_scc1 L_f3o_d%=\n"
    "s_sleep 1\n"
    "ds_read_b32 v126, v125 offset:3072\n"
    "s_branch L_f3o_w%=\n"
    "L_f3o_d%=:\n"
    "ds_read_b64_tr_b16 v[98:99], v160 offset:4096\n"
    "ds_read_b64_tr_b16 v[100:101], v160 offset:8192\n"
    "ds_read_b64_tr_b16 v[102:103], v160 offset:12288\n"
    "ds_read_b64_tr_b16 v[104:105], v160 offset:16384\n"
    "ds_read_b64_tr_b16 v[106:107], v160 offset:0\n"
    "ds_read_b64_tr_b16 v[108:109], v162 offset:1024\n"
    "ds_read_b64_tr_b16 v[84:85], v161 offset:4096\n"
    "ds_read_b64_tr_b16 v[86:87], v161 offset:8192\n"
    "ds_read_b64_tr_b16 v[88:89], v161 offset:12288\n"
    "ds_read_b64_tr_b16 v[90:91], v161 offset:16384\n"
    "ds_read_b64_tr_b16 v[92:93], v161 offset:0\n"
    "ds_read_b64_tr_b16 v[94:95], v162 offset:1056\n"
    "ds_read_b64_tr_b16 v[70:71], v160 offset:4608\n"
    "ds_read_b64_tr_b16 v[72:73], v160 offset:8704\n"
    "ds_read_b64_tr_b16 v[74:75], v160 offset:12800\n"
    "ds_read_b64_tr_b16 v[76:77], v160 offset:16896\n"
    "ds_read_b64_tr_b16 v[78:79], v160 offset:512\n"
    "ds_read_b64_tr_b16 v[80:81], v162 offset:1280\n"
    "ds_read_b64_tr_b16 v[56:57], v161 offset:4608\n"
    "ds_read_b64_tr_b16 v[58:59], v161 offset:8704\n"
    "ds_read_b64_tr_b16 v[60:61], v161 offset:12800\n"
    "ds_read_b64_tr_b16 v[62:63], v161 offset:16896\n"
    "ds_read_b64_tr_b16 v[64:65], v161 offset:512\n"
    "ds_read_b64_tr_b16 v[66:67], v162 offset:1312\n"
    "s_waitcnt lgkmcnt(0)\n"
    "L_b3%=:\n"
    "s_add_u32 s25, s24, 4096\n"
    "s_cmp_eq_u32 s15, 0\n"
    "v_mfma_f32_16x16x32_bf16 v[156:159], v[152:155], v[128:131], 0\n"
    "ds_read_b64_tr_b16 v[42:43], v160 offset:5120\n"
    "ds_read_b64_tr_b16 v[44:45], v160 offset:9216\n"
    "v_mfma_f32_16x16x32_bf16 v[156:159], v[152:155], v[132:135], v[156:159]\n"
    "ds_read_b64_tr_b16 v[46:47], v160 offset:13312\n"
    "ds_read_b64_tr_b16 v[48:49], v160 offset:17408\n"
    "v_mfma_f32_16x16x32_bf16 v[156:159], v[152:155], v[136:139], v[156:159]\n"
    "ds_read_b64_tr_b16 v[50:51], v160 offset:1024\n"
    "ds_read_b64_tr_b16 v[52:53], v162 offset:1536\n"
    "s_waitcnt lgkmcnt(14)\n"
    "v_mfma_f32_16x16x32_bf16 v[140:143], v[98:101], v[112:115], 0\n"
    "ds_read_b64_tr_b16 v[28:29], v161 offset:5120\n"
    "ds_read_b64_tr_b16 v[30:31], v161 offset:9216\n"
    "v_mfma_f32_16x16x32_bf16 v[140:143], v[102:105], v[116:119], v[140:143]\n"
    "ds_read_b64_tr_b16 v[32:33], v161 offset:13312\n"
    "ds_read_b64_tr_b16 v[34:35], v161 offset:17408\n"
    "v_mfma_f32_16x16x32_bf16 v[140:143], v[106:109], v[120:123], v[140:143]\n"
    "ds_read_b64_tr_b16 v[36:37], v161 offset:1024\n"
    "ds_read_b64_tr_b16 v[38:39], v162 offset:1568\n"
    "v_mfma_f32_16x16x32_bf16 v[144:147], v[84:87], v[112:115], 0\n"
    "ds_read_b64_tr_b16 v[14:15], v160 offset:5632\n"
    "ds_read_b64_tr_b16 v[16:17], v160 offset:9728\n"
    "v_mfma_f32_16x16x32_bf16 v[144:147], v[88:91], v[116:119], v[144:147]\n"
    "ds_read_b64_tr_b16 v[18:19], v160 offset:13824\n"
    "ds_read_b64_tr_b16 v[20:21], v160 offset:17920\n"
    "v_mfma_f32_16x16x32_bf16 v[144:147], v[92:95], v[120:123], v[144:147]\n"
    "ds_read_b64_tr_b16 v[22:23], v160 offset:1536\n"
    "ds_read_b64_tr_b16 v[24:25], v162 offset:1792\n"
    "v_rcp_f32 v152, v156\n"
    "v_mov_b32 v154, 0\n"
    "v_mfma_f32_16x16x32_bf16 v[148:151], v[70:73], v[112:115], 0\n"
    "ds_read_b64_tr_b16 v[0:1], v161 offset:5632\n"
    "v_mul_f32 v140, v166, v140\n"
    "v_mul_f32 v141, v166, v141\n"
    "v_mfma_f32_16x16x32_bf16 v[148:151], v[74:77], v[116:119], v[148:151]\n"
    "ds_read_b64_tr_b16 v[2:3], v161 offset:9728\n"
    "v_mul_f32 v142, v166, v142\n"
    "v_mul_f32 v143, v166, v143\n"
    "s_waitcnt lgkmcnt(14)\n"
    "v_mfma_f32_16x16x32_bf16 v[148:151], v[78:81], v[120:123], v[148:151]\n"
    "ds_read_b64_tr_b16 v[4:5], v161 offset:13824\n"
    "v_cvt_pk_bf16_f32 v156, v140, v141\n"
    "v_cvt_pk_bf16_f32 v157, v142, v143\n"
    "v_mfma_f32_16x16x32_bf16 v[140:143], v[56:59], v[112:115], 0\n"
    "ds_write_b64 v163, v[156:157] offset:0\n"
    "v_mul_f32 v144, v166, v144\n"
    "v_mul_f32 v145, v166, v145\n"
    "v_mfma_f32_16x16x32_bf16 v[140:143], v[60:63], v[116:119], v[140:143]\n"
    "ds_read_b64_tr_b16 v[6:7], v161 offset:17920\n"
    "v_mul_f32 v146, v166, v146\n"
    "v_mul_f32 v147, v166, v147\n"
    "v_mfma_f32_16x16x32_bf16 v[140:143], v[64:67], v[120:123], v[140:143]\n"
    "ds_read_b64_tr_b16 v[8:9], v161 offset:1536\n"
    "v_cvt_pk_bf16_f32 v158, v144, v145\n"
    "v_cvt_pk_bf16_f32 v159, v146, v147\n"
    "v_mfma_f32_16x16x32_bf16 v[144:147], v[42:45], v[112:115], 0\n"
    "ds_write_b64 v163, v[158:159] offset:32\n"
    "v_mul_f32 v148, v166, v148\n"
    "v_mul_f32 v149, v166, v149\n"
    "v_mfma_f32_16x16x32_bf16 v[144:147], v[46:49], v[116:119], v[144:147]\n"
    "ds_read_b64_tr_b16 v[10:11], v162 offset:1824\n"
    "v_mul_f32 v150, v166, v150\n"
    "v_mul_f32 v151, v166, v151\n"
    "v_mfma_f32_16x16x32_bf16 v[144:147], v[50:53], v[120:123], v[144:147]\n"
    "ds_read_b64_tr_b16 v[110:111], v160 offset:20480\n"
    "v_cvt_pk_bf16_f32 v156, v148, v149\n"
    "v_cvt_pk_bf16_f32 v157, v150, v151\n"
    "s_waitcnt lgkmcnt(9)\n"
    "v_mfma_f32_16x16x32_bf16 v[148:151], v[28:31], v[112:115], 0\n"
    "ds_write_b64 v163, v[156:157] offset:64\n"
    "v_mul_f32 v140, v166, v140\n"
    "v_mul_f32 v141, v166, v141\n"
    "v_mfma_f32_16x16x32_bf16 v[148:151], v[32:35], v[116:119], v[148:151]\n"
    "ds_read_b64_tr_b16 v[96:97], v161 offset:20480\n"
    "v_mul_f32 v142, v166, v142\n"
    "v_mul_f32 v143, v166, v143\n"
    "v_mfma_f32_16x16x32_bf16 v[148:151], v[36:39], v[120:123], v[148:151]\n"
    "ds_read_b64_tr_b16 v[82:83], v160 offset:20992\n"
    "v_cvt_pk_bf16_f32 v158, v140, v141\n"
    "v_cvt_pk_bf16_f32 v159, v142, v143\n"
    "v_mfma_f32_16x16x32_bf16 v[140:143], v[14:17], v[112:115], 0\n"
    "ds_write_b64 v163, v[158:159] offset:96\n"
    "v_mul_f32 v144, v166, v144\n"
    "v_mul_f32 v145, v166, v145\n"
    "v_mfma_f32_16x16x32_bf16 v[140:143], v[18:21], v[116:119], v[140:143]\n"
    "ds_read_b64_tr_b16 v[68:69], v161 offset:20992\n"
    "v_mul_f32 v146, v166, v146\n"
    "v_mul_f32 v147, v166, v147\n"
    "v_mfma_f32_16x16x32_bf16 v[140:143], v[22:25], v[120:123], v[140:143]\n"
    "ds_read_b64_tr_b16 v[54:55], v160 offset:21504\n"
    "v_cvt_pk_bf16_f32 v156, v144, v145\n"
    "v_cvt_pk_bf16_f32 v157, v146, v147\n"
    "s_waitcnt lgkmcnt(7)\n"
    "v_mfma_f32_16x16x32_bf16 v[144:147], v[0:3], v[112:115], 0\n"
    "ds_write_b64 v163, v[156:157] offset:128\n"
    "v_mul_f32 v148, v166, v148\n"
    "v_mul_f32 v149, v166, v149\n"
    "v_mfma_f32_16x16x32_bf16 v[144:147], v[4:7], v[116:119], v[144:147]\n"
    "ds_read_b64_tr_b16 v[40:41], v161 offset:21504\n"
    "v_mul_f32 v150, v166, v150\n"
    "v_mul_f32 v151, v166, v151\n"
    "v_mfma_f32_16x16x32_bf16 v[144:147], v[8:11], v[120:123], v[144:147]\n"
    "ds_read_b64_tr_b16 v[26:27], v160 offset:22016\n"
    "v_cvt_pk_bf16_f32 v158, v148, v149\n"
    "v_cvt_pk_bf16_f32 v159, v150, v151\n"
    "s_waitcnt lgkmcnt(4)\n"
    "v_mfma_f32_16x16x32_bf16 v[148:151], v[98:101], v[128:131], 0\n"
    "ds_write_b64 v163, v[158:159] offset:160\n"
    "v_mul_f32 v140, v166, v140\n"
    "v_mul_f32 v141, v166, v141\n"
    "v_mfma_f32_16x16x32_bf16 v[148:151], v[102:105], v[132:135], v[148:151]\n"
    "ds_read_b128 v[112:115], v164 offset:0\n"
    "ds_read_b64_tr_b16 v[12:13], v161 offset:22016\n"
    "v_mul_f32 v142, v166, v142\n"
    "v_mul_f32 v143, v166, v143\n"
    "v_mfma_f32_16x16x32_bf16 v[148:151], v[108:111], v[136:139], v[148:151]\n"
    "ds_read_b128 v[116:119], v164 offset:544\n"
    "v_cvt_pk_bf16_f32 v156, v140, v141\n"
    "v_cvt_pk_bf16_f32 v157, v142, v143\n"
    "v_mfma_f32_16x16x32_bf16 v[140:143], v[84:87], v[128:131], 0\n"
    "ds_write_b64 v163, v[156:157] offset:192\n"
    "v_mul_f32 v144, v166, v144\n"
    "v_mul_f32 v145, v166, v145\n"
    "v_mfma_f32_16x16x32_bf16 v[140:143], v[88:91], v[132:135], v[140:143]\n"
    "ds_read_b32 v155, v154 offset:3084\n"
    "v_mul_f32 v146, v166, v146\n"
    "v_mul_f32 v147, v166, v147\n"
    "v_mfma_f32_16x16x32_bf16 v[140:143], v[94:97], v[136:139], v[140:143]\n"
    "v_cvt_pk_bf16_f32 v158, v144, v145\n"
    "v_cvt_pk_bf16_f32 v159, v146, v147\n"
    "v_mfma_f32_16x16x32_bf16 v[144:147], v[70:73], v[128:131], 0\n"
    "ds_write_b64 v163, v[158:159] offset:224\n"
    "v_mul_f32 v148, v152, v148\n"
    "v_mul_f32 v149, v152, v149\n"
    "v_mfma_f32_16x16x32_bf16 v[144:147], v[74:77], v[132:135], v[144:147]\n"
    "ds_read_b128 v[120:123], v164 offset:128\n"
    "v_mul_f32 v150, v152, v150\n"
    "v_mul_f32 v151, v152, v151\n"
    "v_mfma_f32_16x16x32_bf16 v[144:147], v[80:83], v[136:139], v[144:147]\n"
    "ds_read_b128 v[124:127], v164 offset:672\n"
    "s_waitcnt lgkmcnt(3)\n"
    "v_readfirstlane_b32 s13, v155\n"
    "v_cvt_pk_bf16_f32 v156, v148, v149\n"
    "v_cvt_pk_bf16_f32 v157, v150, v151\n"
    "s_min_u32 s13, s13, s15\n"
    "s_cmp_eq_u32 s13, 0\n"
    "v_mfma_f32_16x16x32_bf16 v[148:151], v[56:59], v[128:131], 0\n"
    "ds_write_b64 v163, v[156:157] offset:4352\n"
    "s_cbranch_scc1 L_ys1%=\n"
    "buffer_store_dwordx4 v[112:115], v165, s[48:51], s24 offen offset:0 nt\n"
    "buffer_store_dwordx4 v[116:119], v165, s[48:51], s24 offen offset:512 nt\n"
    "L_ys1%=:\n"
    "v_mul_f32 v140, v152, v140\n"
    "v_mul_f32 v141, v152, v141\n"
    "v_mfma_f32_16x16x32_bf16 v[148:151], v[60:63], v[132:135], v[148:151]\n"
    "v_mul_f32 v142, v152, v142\n"
    "v_mul_f32 v143, v152, v143\n"
    "v_mfma_f32_16x16x32_bf16 v[148:151], v[66:69], v[136:139], v[148:151]\n"
    "v_cvt_pk_bf16_f32 v158, v140, v141\n"
    "v_cvt_pk_bf16_f32 v159, v142, v143\n"
    "v_mfma_f32_16x16x32_bf16 v[140:143], v[42:45], v[128:131], 0\n"
    "ds_write_b64 v163, v[158:159] offset:4384\n"
    "v_mul_f32 v144, v152, v144\n"
    "v_mul_f32 v145, v152, v145\n"
    "v_mfma_f32_16x16x32_bf16 v[140:143], v[46:49], v[132:135], v[140:143]\n"
    "v_mul_f32 v146, v152, v146\n"
    "v_mul_f32 v147, v152, v147\n"
    "v_mfma_f32_16x16x32_bf16 v[140:143], v[52:55], v[136:139], v[140:143]\n"
    "v_cvt_pk_bf16_f32 v156, v144, v145\n"
    "v_cvt_pk_bf16_f32 v157, v146, v147\n"
    "v_mfma_f32_16x16x32_bf16 v[144:147], v[28:31], v[128:131], 0\n"
    "ds_write_b64 v163, v[156:157] offset:4416\n"
    "s_waitcnt lgkmcnt(3)\n"
    "s_cbranch_scc1 L_ys2%=\n"
    "buffer_store_dwordx4 v[120:123], v165, s[48:51], s24 offen offset:128 nt\n"
    "buffer_store_dwordx4 v[124:127], v165, s[48:51], s24 offen offset:640 nt\n"
    "L_ys2%=:\n"
    "v_mul_f32 v148, v152, v148\n"
    "v_mul_f32 v149, v152, v149\n"
    "v_mfma_f32_16x16x32_bf16 v[144:147], v[32:35], v[132:135], v[144:147]\n"
    "v_mul_f32 v150, v152, v150\n"
    "v_mul_f32 v151, v152, v151\n"
    "v_mfma_f32_16x16x32_bf16 v[144:147], v[38:41], v[136:139], v[144:147]\n"
    "v_cvt_pk_bf16_f32 v158, v148, v149\n"
    "v_cvt_pk_bf16_f32 v159, v150, v151\n"
    "v_mfma_f32_16x16x32_bf16 v[148:151], v[14:17], v[128:131], 0\n"
    "ds_write_b64 v163, v[158:159] offset:4448\n"
    "v_mul_f32 v140, v152, v140\n"
    "v_mul_f32 v141, v152, v141\n"
    "v_mfma_f32_16x16x32_bf16 v[148:151], v[18:21], v[132:135], v[148:151]\n"
    "ds_read_b128 v[84:87], v164 offset:4352\n"
    "v_mul_f32 v142, v152, v142\n"
    "v_mul_f32 v143, v152, v143\n"
    "v_mfma_f32_16x16x32_bf16 v[148:151], v[24:27], v[136:139], v[148:151]\n"
    "ds_read_b128 v[88:91], v164 offset:4896\n"
    "v_cvt_pk_bf16_f32 v156, v140, v141\n"
    "v_cvt_pk_bf16_f32 v157, v142, v143\n"
    "v_mfma_f32_16x16x32_bf16 v[140:143], v[0:3], v[128:131], 0\n"
    "ds_write_b64 v163, v[156:157] offset:4480\n"
    "v_mul_f32 v144, v152, v144\n"
    "v_mul_f32 v145, v152, v145\n"
    "v_mfma_f32_16x16x32_bf16 v[140:143], v[4:7], v[132:135], v[140:143]\n"
    "v_mul_f32 v146, v152, v146\n"
    "v_mul_f32 v147, v152, v147\n"
    "v_mfma_f32_16x16x32_bf16 v[140:143], v[10:13], v[136:139], v[140:143]\n"
    "v_cvt_pk_bf16_f32 v158, v144, v145\n"
    "v_cvt_pk_bf16_f32 v159, v146, v147\n"
    "ds_write_b64 v163, v[158:159] offset:4512\n"
    "v_mul_f32 v148, v152, v148\n"
    "v_mul_f32 v149, v152, v149\n"
    "v_mul_f32 v150, v152, v150\n"
    "v_mul_f32 v151, v152, v151\n"
    "v_cvt_pk_bf16_f32 v156, v148, v149\n"
    "v_cvt_pk_bf16_f32 v157, v150, v151\n"
    "v_mul_f32 v140, v152, v140\n"
    "v_mul_f32 v141, v152, v141\n"
    "v_mul_f32 v142, v152, v142\n"
    "v_mul_f32 v143, v152, v143\n"
    "ds_write_b64 v163, v[156:157] offset:4544\n"
    "v_cvt_pk_bf16_f32 v158, v140, v141\n"
    "v_cvt_pk_bf16_f32 v159, v142, v143\n"
    "ds_write_b64 v163, v[158:159] offset:4576\n"
    "ds_read_b128 v[92:95], v164 offset:4480\n"
    "ds_read_b128 v[96:99], v164 offset:5024\n"
    "s_waitcnt lgkmcnt(6)\n"
    "s_cbranch_scc1 L_ys3%=\n"
    "buffer_store_dwordx4 v[84:87], v165, s[48:51], s25 offen offset:0 nt\n"
    "buffer_store_dwordx4 v[88:91], v165, s[48:51], s25 offen offset:512 nt\n"
    "L_ys3%=:\n"
    "s_cbranch_scc0 L_b5%=\n"
    "s_cmp_eq_u32 s15, 0\n"
    "s_cbranch_scc1 L_b6%=\n"
    "v_mov_b32 v128, 0\n"
    "L_f6y_r%=:\n"
    "ds_read_b32 v129, v128 offset:3084\n"
    "L_f6y_w%=:\n"
    "s_waitcnt lgkmcnt(0)\n"
    "v_readfirstlane_b32 s13, v129\n"
    "s_nop 1\n"
    "s_cmp_ge_u32 s13, 1\n"
    "s_cbranch_scc1 L_f6y_d%=\n"
    "s_sleep 1\n"
    "ds_read_b32 v129, v128 offset:3084\n"
    "s_branch L_f6y_w%=\n"
    "L_f6y_d%=:\n"
    "L_b6%=:\n"
    "s_cmp_eq_u32 s15, 0\n"
    "s_cbranch_scc0 L_b7%=\n"
    "s_setprio 2\n"
    "v_writelane_b32 v130, s40, 0\n"
    "v_writelane_b32 v130, s41, 1\n"
    "v_writelane_b32 v130, s42, 2\n"
    "v_writelane_b32 v130, s43, 3\n"
    "v_writelane_b32 v130, s16, 4\n"
    "v_writelane_b32 v130, s17, 5\n"
    "v_writelane_b32 v130, s20, 6\n"
    "v_writelane_b32 v130, s21, 7\n"
    "v_mov_b32 v128, 0\n"
    "L_f5o_r%=:\n"
    "ds_read_b32 v129, v128 offset:3080\n"
    "L_f5o_w%=:\n"
    "s_waitcnt lgkmcnt(0)\n"
    "v_readfirstlane_b32 s13, v129\n"
    "s_nop 1\n"
    "s_cmp_ge_u32 s13, 4\n"
    "s_cbranch_scc1 L_f5o_d%=\n"
    "s_sleep 1\n"
    "ds_read_b32 v129, v128 offset:3080\n"
    "s_branch L_f5o_w%=\n"
    "L_f5o_d%=:\n"
    "s_waitcnt vmcnt(0)\n"
    "v_cmp_ne_u32 vcc, v130, v167\n"
    "v_subrev_u32 v128, s8, v167\n"
    "v_max_i32 v128, 0, v128\n"
    "v_min_u32 v128, 250, v128\n"
    "v_cndmask_b32 v128, 0, v128, vcc\n"
    "s_nop 1\n"
    "v_max_u32_dpp v128, v128, v128 quad_perm:[1,0,3,2] row_mask:0xf bank_mask:0xf\n"
    "s_nop 1\n"
    "v_max_u32_dpp v128, v128, v128 quad_perm:[2,3,0,1] row_mask:0xf bank_mask:0xf\n"
    "s_nop 1\n"
    "v_max_u32_dpp v128, v128, v128 row_half_mirror row_mask:0xf bank_mask:0xf\n"
    "s_nop 0\n"
    "v_readfirstlane_b32 s13, v128\n"
    "s_nop 1\n"
    "s_sub_u32 s13, s13, 20\n"
    "s_max_i32 s13, s13, 0\n"
    "s_add_u32 s8, s8, s13\n"
    "s_memrealtime s[22:23]\n"
    "s_waitcnt lgkmcnt(0)\n"
    "s_sub_u32 s13, s22, s8\n"
    "s_add_u32 s13, s13, 200\n"
    "s_min_i32 s13, s13, 790\n"
    "L_tg%=:\n"
    "s_memrealtime s[22:23]\n"
    "s_waitcnt lgkmcnt(0)\n"
    "s_sub_u32 s22, s22, s8\n"
    "s_cmp_lt_i32 s22, s13\n"
    "s_cbranch_scc1 L_tg%=\n"
    "s_mov_b64 exec, 1\n"
    "v_mov_b32 v128, 0\n"
    "v_mov_b32 v129, 1\n"
    "ds_add_u32 v128, v129 offset:3084\n"
    "s_mov_b64 exec, -1\n"
    "L_b7%=:\n"
    "s_cmp_eq_u32 s24, 0\n"
    "s_cbranch_scc1 L_st0%=\n"
    "buffer_store_dwordx4 v[112:115], v165, s[48:51], s24 offen offset:0 nt\n"
    "buffer_store_dwordx4 v[116:119], v165, s[48:51], s24 offen offset:512 nt\n"
    "buffer_store_dwordx4 v[120:123], v165, s[48:51], s24 offen offset:128 nt\n"
    "buffer_store_dwordx4 v[124:127], v165, s[48:51], s24 offen offset:640 nt\n"
    "L_stb%=:\n"
    "buffer_store_dwordx4 v[84:87], v165, s[48:51], s25 offen offset:0 nt\n"
    "buffer_store_dwordx4 v[88:91], v165, s[48:51], s25 offen offset:512 nt\n"
    "L_b5%=:\n"
    "s_waitcnt lgkmcnt(1)\n"
    "buffer_store_dwordx4 v[92:95], v165, s[48:51], s25 offen offset:128 nt\n"
    "s_waitcnt lgkmcnt(0)\n"
    "buffer_store_dwordx4 v[96:99], v165, s[48:51], s25 offen offset:640 nt\n"
    "s_endpgm\n"
    "L_st0%=:\n"
    "s_lshl_b32 s13, s31, 8\n"
    "v_bfrev_b32 v132, 1\n"
    "v_add_u32 v128, 0, v165\n"
    "v_cmp_le_u32 vcc, s13, v128\n"
    "s_nop 1\n"
    "v_cndmask_b32 v128, v132, v128, vcc\n"
    "buffer_store_dwordx4 v[112:115], v128, s[48:51], 0 offen offset:0 nt\n"
    "buffer_store_dwordx4 v[120:123], v128, s[48:51], 0 offen offset:128 nt\n"
    "v_add_u32 v129, 512, v165\n"
    "v_cmp_le_u32 vcc, s13, v129\n"
    "s_nop 1\n"
    "v_cndmask_b32 v129, v132, v129, vcc\n"
    "buffer_store_dwordx4 v[116:119], v129, s[48:51], 0 offen offset:0 nt\n"
    "buffer_store_dwordx4 v[124:127], v129, s[48:51], 0 offen offset:128 nt\n"
    "s_branch L_stb%=\n"
    "L_helper%=:\n"
    "s_setprio 3\n"
    "s_cmp_ge_u32 s33, 10\n"
    "s_cbranch_scc0 L_nocpf%=\n"
    "s_cmp_eq_u32 s34, 112\n"
    "s_cbranch_scc0 L_nocpf%=\n"
    "s_getpc_b64 s[64:65]\n"
    "L_pc%=:\n"
    "s_sub_u32 s64, s64, L_pc%=-L_begin%=\n"
    "s_subb_u32 s65, s65, 0\n"
    "s_and_b32 s65, s65, 0xffff\n"
    "s_mov_b32 s66, L_fb%=-L_begin%=\n"
    "s_mov_b32 s67, 0x20000\n"
    "s_sub_u32 s13, s33, 10\n"
    "s_lshl_b32 s13, s13, 13\n"
    "v_lshl_add_u32 v41, v47, 7, s13\n"
    "buffer_load_dword v41, v41, s[64:67], 0 offen\n"
    "L_nocpf%=:\n"
    "v_mov_b32 v166, v47\n"
    "s_bfe_u32 s14, s33, 0x10001\n"
    "s_and_b32 s15, s33, 1\n"
    "v_bfe_u32 v159, v47, 2, 3\n"
    "v_lshrrev_b32 v160, 5, v47\n"
    "v_lshlrev_b32 v160, 6, v160\n"
    "v_lshl_or_b32 v160, v159, 8, v160\n"
    "s_lshl_b32 s15, s15, 7\n"
    "s_lshl_b32 s13, s14, 11\n"
    "s_or_b32 s15, s15, s13\n"
    "v_or_b32 v160, s15, v160\n"
    "v_and_b32 v163, 3, v47\n"
    "v_lshrrev_b32 v159, 1, v159\n"
    "v_and_b32 v159, 2, v159\n"
    "v_xor_b32 v163, v159, v163\n"
    "v_lshl_or_b32 v163, v163, 4, v160\n"
    "v_and_b32 v160, 3, v48\n"
    "v_lshrrev_b32 v164, 2, v48\n"
    "v_lshlrev_b32 v164, 8, v164\n"
    "v_lshl_or_b32 v164, v49, 6, v164\n"
    "v_lshl_or_b32 v164, v160, 4, v164\n"
    "s_lshr_b32 s52, s34, 4\n"
    "s_sub_u32 s56, s33, 8\n"
    "s_or_b32 s57, s52, s56\n"
    "s_lshl_b32 s13, s35, 19\n"
    "s_waitcnt lgkmcnt(0)\n"
    "s_load_dword s30, s[22:23], 0x0\n"
    "s_load_dword s31, s[24:25], 0x0\n"
    "s_mul_i32 s13, s35, 20608\n"
    "s_add_u32 s60, s28, s13\n"
    "s_addc_u32 s61, s29, 0\n"
    "s_and_b32 s61, s61, 0xffff\n"
    "s_mov_b32 s62, 20608\n"
    "s_mov_b32 s63, 0x20000\n"
    "buffer_load_dword v156, off, s[60:63], 0 sc1\n"
    "s_lshl_b32 s13, s35, 19\n"
    "s_add_u32 s44, s16, s13\n"
    "s_addc_u32 s45, s17, 0\n"
    "s_and_b32 s45, s45, 0xffff\n"
    "s_mov_b32 s46, 0x80000\n"
    "s_mov_b32 s47, 0x20000\n"
    "s_add_u32 s36, s18, s13\n"
    "s_addc_u32 s37, s19, 0\n"
    "s_and_b32 s37, s37, 0xffff\n"
    "s_mov_b32 s38, 0x80000\n"
    "s_mov_b32 s39, 0x20000\n"
    "s_add_u32 s40, s20, s13\n"
    "s_addc_u32 s41, s21, 0\n"
    "s_and_b32 s41, s41, 0xffff\n"
    "s_mov_b32 s42, 0x80000\n"
    "s_mov_b32 s43, 0x20000\n"
    "s_add_u32 s48, s26, s13\n"
    "s_addc_u32 s49, s27, 0\n"
    "s_and_b32 s49, s49, 0xffff\n"
    "s_mov_b32 s50, 0x80000\n"
    "s_mov_b32 s51, 0x20000\n"
    "s_cmp_eq_u32 s56, 1\n"
    "s_cbranch_scc0 L_nokg%=\n"
    "s_mov_b32 m0, 0\n"
    "s_nop 0\n"
    "buffer_load_dwordx4 v164, s[36:39], 0 offen lds\n"
    "s_mov_b32 m0, 1024\n"
    "s_nop 0\n"
    "buffer_load_dwordx4 v164, s[40:43], 0 offen lds\n"
    "s_mov_b32 m0, 2048\n"
    "s_nop 0\n"
    "buffer_load_dwordx4 v164, s[44:47], 0 offen lds\n"
    "L_nokg%=:\n"
    "v_lshrrev_b32 v159, 3, v48\n"
    "v_mul_u32_u24 v152, 0x810, v159\n"
    "v_and_b32 v159, 7, v48\n"
    "v_lshl_or_b32 v152, v159, 6, v152\n"
    "v_and_b32 v159, 1, v49\n"
    "v_lshl_or_b32 v152, v159, 5, v152\n"
    "v_lshrrev_b32 v160, 1, v49\n"
    "v_lshl_or_b32 v152, v160, 9, v152\n"
    "v_mul_u32_u24 v154, 0x120, v159\n"
    "v_lshl_or_b32 v154, v160, 11, v154\n"
    "v_lshrrev_b32 v159, 2, v48\n"
    "v_lshl_or_b32 v154, v159, 6, v154\n"
    "v_and_b32 v159, 3, v48\n"
    "v_lshl_or_b32 v154, v159, 3, v154\n"
    "v_lshlrev_b32 v38, 6, v159\n"
    "v_lshl_or_b32 v38, v160, 8, v38\n"
    "v_and_b32 v160, 1, v49\n"
    "v_lshl_or_b32 v38, v160, 5, v38\n"
    "s_lshl_b32 s13, s56, 14\n"
    "s_add_u32 s13, s13, 12288\n"
    "v_add_u32 v152, s13, v152\n"
    "v_xor_b32 v153, 16, v152\n"
    "s_lshl_b32 s13, s56, 9\n"
    "s_add_u32 s13, s13, 90112\n"
    "v_add_u32 v154, s13, v154\n"
    "v_xor_b32 v155, 32, v154\n"
    "v_and_b32 v159, 3, v48\n"
    "v_lshlrev_b32 v165, 4, v159\n"
    "v_lshl_or_b32 v165, v49, 6, v165\n"
    "s_mul_i32 s13, s52, 640\n"
    "s_add_u32 s13, s13, 128\n"
    "v_mul_u32_u24 v162, 5120, v48\n"
    "v_add_u32 v162, s13, v162\n"
    "v_lshl_add_u32 v161, v49, 4, v162\n"
    "s_lshl_b32 s13, s56, 7\n"
    "v_add_u32 v161, s13, v161\n"
    "s_lshl_b32 s13, s56, 24\n"
    "v_add_u32 v162, s13, v162\n"
    "v_mov_b32 v148, 0x3f803f80\n"
    "v_mov_b32 v149, 0x3f803f80\n"
    "v_mov_b32 v150, 0x3f803f80\n"
    "v_mov_b32 v151, 0x3f803f80\n"
    "v_mov_b32 v0, 0\n"
    "v_mov_b32 v1, 0\n"
    "v_mov_b32 v2, 0\n"
    "v_mov_b32 v3, 0\n"
    "ds_write_b128 v0, v[0:3] offset:3072\n"
    "s_waitcnt lgkmcnt(0)\n"
    "s_cmp_gt_u32 s30, 32\n"
    "s_cbranch_scc1 L_fb%=\n"
    "s_cmp_gt_u32 s31, 4\n"
    "s_cbranch_scc1 L_fb%=\n"
    "s_mul_i32 s13, s31, 5120\n"
    "s_add_u32 s62, s13, 128\n"
    "v_mov_b32 v160, 1\n"
    "v_cmp_gt_u32_e64 s[58:59], s31, v48\n"
    "s_nop 1\n"
    "s_mov_b64 exec, s[58:59]\n"
    "s_cbranch_execz L_xw1%=\n"
    "buffer_load_dwordx4 v[50:53], v161, s[60:63], 0 offen offset:0 sc1\n"
    "buffer_load_dwordx4 v[54:57], v161, s[60:63], 0 offen offset:64 sc1\n"
    "s_cmp_eq_u32 s56, 0\n"
    "s_cbranch_scc0 L_xw1%=\n"
    "buffer_load_dwordx2 v[58:59], v162, s[60:63], 0 offen offset:512 sc1\n"
    "L_xw1%=:\n"
    "s_mov_b64 exec, -1\n"
    "s_cmp_eq_u32 s57, 0\n"
    "s_cbranch_scc0 L_noclr%=\n"
    "v_lshrrev_b32 v24, 5, v166\n"
    "v_mul_u32_u24 v22, 5120, v24\n"
    "v_add_u32 v22, 128, v22\n"
    "v_and_b32 v24, 31, v166\n"
    "v_lshl_add_u32 v20, v24, 4, v22\n"
    "s_call_b64 s[68:69], L_gather%=\n"
    "v_mov_b32 v60, v25\n"
    "v_mov_b32 v61, v26\n"
    "s_cmp_gt_u32 s31, 2\n"
    "s_cbranch_scc0 L_noclr%=\n"
    "v_add_u32 v20, 10240, v20\n"
    "v_add_u32 v22, 10240, v22\n"
    "s_call_b64 s[68:69], L_gather%=\n"
    "v_mov_b32 v62, v25\n"
    "v_mov_b32 v63, v26\n"
    "L_noclr%=:\n"
    "s_setprio 0\n"
    "s_barrier\n"
    "s_bitcmp1_b32 s34, 4\n"
    "s_cselect_b32 s20, -1, 1\n"
    "s_cselect_b32 s21, 77824, 0\n"
    "s_lshl_b32 s14, s56, 10\n"
    "s_add_u32 s14, s14, s21\n"
    "s_add_u32 s14, s14, 81920\n"
    "s_lshl_b32 s15, s34, 12\n"
    "s_add_u32 s15, s15, s21\n"
    "s_add_u32 s15, s15, -8192\n"
    "s_mul_i32 s13, s20, 0\n"
    "s_add_u32 m0, s14, s13\n"
    "s_add_u32 s13, s13, s15\n"
    "s_min_u32 s13, s13, 0x40000000\n"
    "v_add_u32 v164, s13, v163\n"
    "buffer_load_dwordx4 v164, s[40:43], 0 offen nt lds\n"
    "s_mul_i32 s13, s20, 12288\n"
    "s_add_u32 m0, s14, s13\n"
    "s_add_u32 s13, s13, s15\n"
    "v_add_u32 v164, s13, v163\n"
    "buffer_load_dwordx4 v164, s[40:43], 0 offen nt lds\n"
    "s_mul_i32 s13, s20, 16384\n"
    "s_add_u32 m0, s14, s13\n"
    "s_add_u32 s13, s13, s15\n"
    "v_add_u32 v164, s13, v163\n"
    "buffer_load_dwordx4 v164, s[40:43], 0 offen nt lds\n"
    "s_mul_i32 s13, s20, 20480\n"
    "s_add_u32 m0, s14, s13\n"
    "s_add_u32 s13, s13, s15\n"
    "v_add_u32 v164, s13, v163\n"
    "buffer_load_dwordx4 v164, s[40:43], 0 offen nt lds\n"
    "s_mul_i32 s13, s20, 24576\n"
    "s_add_u32 m0, s14, s13\n"
    "s_add_u32 s13, s13, s15\n"
    "v_add_u32 v164, s13, v163\n"
    "buffer_load_dwordx4 v164, s[40:43], 0 offen nt lds\n"
    "s_mul_i32 s13, s20, 28672\n"
    "s_add_u32 m0, s14, s13\n"
    "s_add_u32 s13, s13, s15\n"
    "v_add_u32 v164, s13, v163\n"
    "buffer_load_dwordx4 v164, s[40:43], 0 offen nt lds\n"
    "s_waitcnt vmcnt(6)\n"
    "s_barrier\n"
    "s_cmp_eq_u32 s57, 0\n"
    "s_cbranch_scc0 L_nob1%=\n"
    "s_mov_b64 exec, 1\n"
    "buffer_atomic_add v160, off, s[60:63], 0\n"
    "s_mov_b64 exec, -1\n"
    "L_nob1%=:\n"
    "s_setprio 2\n"
    "s_mul_i32 s13, s20, 4096\n"
    "s_add_u32 m0, s14, s13\n"
    "s_add_u32 s13, s13, s15\n"
    "s_min_u32 s13, s13, 0x40000000\n"
    "v_add_u32 v164, s13, v163\n"
    "buffer_load_dwordx4 v164, s[40:43], 0 offen nt lds\n"
    "ds_read_b128 v[128:131], v38 offset:2048\n"
    "ds_read_b128 v[132:135], v38 offset:2064\n"
    "ds_read_b128 v[136:139], v38 offset:2560\n"
    "ds_read_b128 v[140:143], v38 offset:2576\n"
    "ds_read_b128 v[64:67], v152 offset:0\n"
    "ds_read_b128 v[68:71], v153 offset:0\n"
    "ds_read_b128 v[72:75], v152 offset:1024\n"
    "ds_read_b128 v[76:79], v153 offset:1024\n"
    "ds_read_b128 v[80:83], v152 offset:4096\n"
    "ds_read_b128 v[84:87], v153 offset:4096\n"
    "ds_read_b128 v[88:91], v152 offset:5120\n"
    "ds_read_b128 v[92:95], v153 offset:5120\n"
    "s_mul_i32 s13, s20, 8192\n"
    "s_add_u32 m0, s14, s13\n"
    "s_add_u32 s13, s13, s15\n"
    "v_add_u32 v164, s13, v163\n"
    "buffer_load_dwordx4 v164, s[40:43], 0 offen nt lds\n"
    "ds_read_b128 v[96:99], v152 offset:8192\n"
    "ds_read_b128 v[100:103], v153 offset:8192\n"
    "ds_read_b128 v[104:107], v152 offset:9216\n"
    "ds_read_b128 v[108:111], v153 offset:9216\n"
    "ds_read_b128 v[112:115], v152 offset:12288\n"
    "ds_read_b128 v[116:119], v153 offset:12288\n"
    "ds_read_b128 v[120:123], v152 offset:13312\n"
    "ds_read_b128 v[124:127], v153 offset:13312\n"
    "s_mul_i32 s13, s20, 32768\n"
    "s_add_u32 m0, s14, s13\n"
    "s_add_u32 s13, s13, s15\n"
    "v_add_u32 v164, s13, v163\n"
    "buffer_load_dwordx4 v164, s[40:43], 0 offen nt lds\n"
    "s_mul_i32 s13, s20, 36864\n"
    "s_add_u32 m0, s14, s13\n"
    "s_add_u32 s13, s13, s15\n"
    "v_add_u32 v164, s13, v163\n"
    "buffer_load_dwordx4 v164, s[40:43], 0 offen nt lds\n"
    "s_mul_i32 s13, s20, 40960\n"
    "s_add_u32 m0, s14, s13\n"
    "s_add_u32 s13, s13, s15\n"
    "v_add_u32 v164, s13, v163\n"
    "buffer_load_dwordx4 v164, s[40:43], 0 offen nt lds\n"
    "s_waitcnt lgkmcnt(0)\n"
    "s_barrier\n"
    "s_setprio 3\n"
    "s_mul_i32 s13, s20, 45056\n"
    "s_add_u32 m0, s14, s13\n"
    "s_add_u32 s13, s13, s15\n"
    "v_add_u32 v164, s13, v163\n"
    "buffer_load_dwordx4 v164, s[40:43], 0 offen nt lds\n"
    "s_setprio 2\n"
    "s_waitcnt lgkmcnt(0)\n"
    "v_mfma_f32_16x16x32_bf16 v[0:3], v[64:67], v[128:131], 0\n"
    "v_mfma_f32_16x16x32_bf16 v[0:3], v[68:71], v[132:135], v[0:3]\n"
    "v_mfma_f32_16x16x32_bf16 v[0:3], v[72:75], v[136:139], v[0:3]\n"
    "v_mfma_f32_16x16x32_bf16 v[0:3], v[76:79], v[140:143], v[0:3]\n"
    "v_mfma_f32_16x16x32_bf16 v[4:7], v[80:83], v[128:131], 0\n"
    "v_mfma_f32_16x16x32_bf16 v[4:7], v[84:87], v[132:135], v[4:7]\n"
    "v_mfma_f32_16x16x32_bf16 v[4:7], v[88:91], v[136:139], v[4:7]\n"
    "v_mfma_f32_16x16x32_bf16 v[4:7], v[92:95], v[140:143], v[4:7]\n"
    "v_mfma_f32_16x16x32_bf16 v[8:11], v[96:99], v[128:131], 0\n"
    "v_mfma_f32_16x16x32_bf16 v[8:11], v[100:103], v[132:135], v[8:11]\n"
    "v_mfma_f32_16x16x32_bf16 v[8:11], v[104:107], v[136:139], v[8:11]\n"
    "v_mfma_f32_16x16x32_bf16 v[8:11], v[108:111], v[140:143], v[8:11]\n"
    "v_mfma_f32_16x16x32_bf16 v[12:15], v[112:115], v[128:131], 0\n"
    "v_mfma_f32_16x16x32_bf16 v[12:15], v[116:119], v[132:135], v[12:15]\n"
    "v_mfma_f32_16x16x32_bf16 v[12:15], v[120:123], v[136:139], v[12:15]\n"
    "v_mfma_f32_16x16x32_bf16 v[12:15], v[124:127], v[140:143], v[12:15]\n"
    "v_max_f32 v158, v0, v1\n"
    "v_max_f32 v159, v2, v3\n"
    "v_max3_f32 v158, v158, v4, v5\n"
    "v_max3_f32 v159, v159, v6, v7\n"
    "v_max3_f32 v158, v158, v8, v9\n"
    "v_max3_f32 v159, v159, v10, v11\n"
    "s_nop 1\n"
    "v_max3_f32 v158, v158, v12, v13\n"
    "v_max3_f32 v159, v159, v14, v15\n"
    "v_max_f32 v158, v158, v159\n"
    "v_mov_b32 v159, v158\n"
    "s_nop 1\n"
    "v_permlane16_swap_b32 v158, v159\n"
    "v_max_f32 v158, v158, v159\n"
    "v_mov_b32 v159, v158\n"
    "s_nop 1\n"
    "v_permlane32_swap_b32 v158, v159\n"
    "v_max_f32 v158, v158, v159\n"
    "v_mul_f32 v159, 0xbe0293ee, v158\n"
    "v_mul_f32 v158, 0x3e0293ee, v158\n"
    "v_fmamk_f32 v0, v0, 0x3e0293ee, v159\n"
    "v_fmamk_f32 v1, v1, 0x3e0293ee, v159\n"
    "v_fmamk_f32 v2, v2, 0x3e0293ee, v159\n"
    "v_fmamk_f32 v3, v3, 0x3e0293ee, v159\n"
    "v_fmamk_f32 v4, v4, 0x3e0293ee, v159\n"
    "v_fmamk_f32 v5, v5, 0x3e0293ee, v159\n"
    "v_fmamk_f32 v6, v6, 0x3e0293ee, v159\n"
    "v_fmamk_f32 v7, v7, 0x3e0293ee, v159\n"
    "v_fmamk_f32 v8, v8, 0x3e0293ee, v159\n"
    "v_fmamk_f32 v9, v9, 0x3e0293ee, v159\n"
    "v_fmamk_f32 v10, v10, 0x3e0293ee, v159\n"
    "v_fmamk_f32 v11, v11, 0x3e0293ee, v159\n"
    "v_fmamk_f32 v12, v12, 0x3e0293ee, v159\n"
    "v_fmamk_f32 v13, v13, 0x3e0293ee, v159\n"
    "v_fmamk_f32 v14, v14, 0x3e0293ee, v159\n"
    "v_fmamk_f32 v15, v15, 0x3e0293ee, v159\n"
    "v_exp_f32 v0, v0\n"
    "v_exp_f32 v1, v1\n"
    "v_exp_f32 v2, v2\n"
    "v_cvt_pk_bf16_f32 v0, v0, v1\n"
    "v_exp_f32 v3, v3\n"
    "v_exp_f32 v4, v4\n"
    "v_cvt_pk_bf16_f32 v1, v2, v3\n"
    "v_exp_f32 v5, v5\n"
    "v_exp_f32 v6, v6\n"
    "v_cvt_pk_bf16_f32 v2, v4, v5\n"
    "v_exp_f32 v7, v7\n"
    "v_exp_f32 v8, v8\n"
    "v_cvt_pk_bf16_f32 v3, v6, v7\n"
    "v_exp_f32 v9, v9\n"
    "v_exp_f32 v10, v10\n"
    "v_cvt_pk_bf16_f32 v4, v8, v9\n"
    "v_exp_f32 v11, v11\n"
    "v_exp_f32 v12, v12\n"
    "v_cvt_pk_bf16_f32 v5, v10, v11\n"
    "v_exp_f32 v13, v13\n"
    "v_exp_f32 v14, v14\n"
    "v_cvt_pk_bf16_f32 v6, v12, v13\n"
    "v_exp_f32 v15, v15\n"
    "s_nop 0\n"
    "v_cvt_pk_bf16_f32 v7, v14, v15\n"
    "v_mfma_f32_16x16x32_bf16 v[144:147], v[148:151], v[0:3], 0\n"
    "s_nop 0\n"
    "v_mfma_f32_16x16x32_bf16 v[144:147], v[148:151], v[4:7], v[144:147]\n"
    "s_lshl_b32 s13, s56, 9\n"
    "v_add_u32 v36, s13, v165\n"
    "s_lshl_b32 s13, s56, 5\n"
    "v_lshl_add_u32 v37, v166, 3, s13\n"
    "s_mov_b32 exec_lo, 0x000f000f\n"
    "s_mov_b32 exec_hi, 0x000f000f\n"
    "ds_write_b128 v36, v[0:3] offset:4096\n"
    "ds_write_b128 v36, v[4:7] offset:4352\n"
    "s_mov_b64 exec, 15\n"
    "v_mov_b32 v159, v144\n"
    "ds_write_b64 v37, v[158:159] offset:6144\n"
    "s_mov_b64 exec, -1\n"
    "v_not_b32 v128, v50\n"
    "v_not_b32 v129, v51\n"
    "v_not_b32 v130, v52\n"
    "v_not_b32 v131, v53\n"
    "v_not_b32 v132, v54\n"
    "v_not_b32 v133, v55\n"
    "v_not_b32 v134, v56\n"
    "v_not_b32 v135, v57\n"
    "v_not_b32 v144, v58\n"
    "v_not_b32 v145, v59\n"
    "v_mov_b32 v146, v60\n"
    "v_mov_b32 v147, v61\n"
    "v_mov_b32 v148, v62\n"
    "v_mov_b32 v149, v63\n"
    "s_setprio 3\n"
    "s_waitcnt vmcnt(0)\n"
    "buffer_load_dword v157, off, s[60:63], 0 sc1\n"
    "v_and_b32 v163, 3, v166\n"
    "v_lshlrev_b32 v163, 3, v163\n"
    "s_waitcnt lgkmcnt(0)\n"
    "s_mov_b64 exec, 1\n"
    "v_mov_b32 v16, 0\n"
    "v_mov_b32 v17, 1\n"
    "ds_add_u32 v16, v17 offset:3072\n"
    "s_mov_b64 exec, -1\n"
    "s_mov_b64 exec, 1\n"
    "v_mov_b32 v16, 0\n"
    "v_mov_b32 v17, 1\n"
    "ds_add_u32 v16, v17 offset:3076\n"
    "s_mov_b64 exec, -1\n"
    "v_mov_b32 v16, 0\n"
    "L_f3h_r%=:\n"
    "ds_read_b32 v17, v16 offset:3072\n"
    "L_f3h_w%=:\n"
    "s_waitcnt lgkmcnt(0)\n"
    "v_readfirstlane_b32 s13, v17\n"
    "s_nop 1\n"
    "s_cmp_ge_u32 s13, 4\n"
    "s_cbranch_scc1 L_f3h_d%=\n"
    "s_sleep 1\n"
    "ds_read_b32 v17, v16 offset:3072\n"
    "s_branch L_f3h_w%=\n"
    "L_f3h_d%=:\n"
    "s_setprio 2\n"
    "ds_read_b128 v[0:3], v165 offset:4096\n"
    "ds_read_b128 v[4:7], v165 offset:4352\n"
    "ds_read_b128 v[8:11], v165 offset:4608\n"
    "ds_read_b128 v[12:15], v165 offset:4864\n"
    "ds_read_b128 v[16:19], v165 offset:5120\n"
    "ds_read_b128 v[20:23], v165 offset:5376\n"
    "ds_read_b128 v[24:27], v165 offset:5632\n"
    "ds_read_b128 v[28:31], v165 offset:5888\n"
    "ds_read_b64 v[136:137], v163 offset:6144\n"
    "ds_read_b64 v[138:139], v163 offset:6176\n"
    "ds_read_b64 v[140:141], v163 offset:6208\n"
    "ds_read_b64 v[142:143], v163 offset:6240\n"
    "ds_read_b64_tr_b16 v[88:89], v154 offset:24576\n"
    "ds_read_b64_tr_b16 v[90:91], v154 offset:28672\n"
    "ds_read_b64_tr_b16 v[92:93], v155 offset:24576\n"
    "ds_read_b64_tr_b16 v[94:95], v155 offset:28672\n"
    "ds_read_b64_tr_b16 v[96:97], v154 offset:32768\n"
    "ds_read_b64_tr_b16 v[98:99], v154 offset:36864\n"
    "ds_read_b64_tr_b16 v[100:101], v155 offset:32768\n"
    "ds_read_b64_tr_b16 v[102:103], v155 offset:36864\n"
    "s_waitcnt lgkmcnt(9)\n"
    "v_max3_f32 v158, v136, v138, v140\n"
    "s_waitcnt lgkmcnt(8)\n"
    "v_max_f32 v158, v158, v142\n"
    "v_sub_f32 v136, v136, v158\n"
    "v_sub_f32 v138, v138, v158\n"
    "v_sub_f32 v140, v140, v158\n"
    "v_sub_f32 v142, v142, v158\n"
    "v_exp_f32 v136, v136\n"
    "v_exp_f32 v138, v138\n"
    "v_exp_f32 v140, v140\n"
    "v_exp_f32 v142, v142\n"
    "v_mul_f32 v159, v136, v137\n"
    "v_fmac_f32 v159, v138, v139\n"
    "v_fmac_f32 v159, v140, v141\n"
    "v_fmac_f32 v159, v142, v143\n"
    "s_waitcnt lgkmcnt(6)\n"
    "v_mfma_f32_16x16x32_bf16 v[40:43], v[88:91], v[12:15], 0\n"
    "s_waitcnt lgkmcnt(4)\n"
    "v_mfma_f32_16x16x32_bf16 v[44:47], v[92:95], v[12:15], 0\n"
    "s_waitcnt lgkmcnt(2)\n"
    "v_mfma_f32_16x16x32_bf16 v[48:51], v[96:99], v[16:19], 0\n"
    "s_waitcnt lgkmcnt(0)\n"
    "v_mfma_f32_16x16x32_bf16 v[52:55], v[100:103], v[16:19], 0\n"
    "v_bfi_b32 v158, 1, v144, v158\n"
    "v_bfi_b32 v159, 1, v145, v159\n"
    "v_mov_b32 v150, 0\n"
    "L_f4h_r%=:\n"
    "ds_read_b32 v151, v150 offset:3076\n"
    "L_f4h_w%=:\n"
    "s_waitcnt lgkmcnt(0)\n"
    "v_readfirstlane_b32 s13, v151\n"
    "s_nop 1\n"
    "s_cmp_ge_u32 s13, 8\n"
    "s_cbranch_scc1 L_f4h_d%=\n"
    "s_sleep 1\n"
    "ds_read_b32 v151, v150 offset:3076\n"
    "s_branch L_f4h_w%=\n"
    "L_f4h_d%=:\n"
    "ds_read_b64_tr_b16 v[64:65], v154 offset:0\n"
    "ds_read_b64_tr_b16 v[66:67], v154 offset:4096\n"
    "ds_read_b64_tr_b16 v[68:69], v155 offset:0\n"
    "ds_read_b64_tr_b16 v[70:71], v155 offset:4096\n"
    "ds_read_b64_tr_b16 v[72:73], v154 offset:8192\n"
    "ds_read_b64_tr_b16 v[74:75], v154 offset:12288\n"
    "ds_read_b64_tr_b16 v[76:77], v155 offset:8192\n"
    "ds_read_b64_tr_b16 v[78:79], v155 offset:12288\n"
    "ds_read_b64_tr_b16 v[80:81], v154 offset:16384\n"
    "ds_read_b64_tr_b16 v[82:83], v154 offset:20480\n"
    "ds_read_b64_tr_b16 v[84:85], v155 offset:16384\n"
    "ds_read_b64_tr_b16 v[86:87], v155 offset:20480\n"
    "ds_read_b64_tr_b16 v[104:105], v154 offset:40960\n"
    "ds_read_b64_tr_b16 v[106:107], v154 offset:45056\n"
    "ds_read_b64_tr_b16 v[108:109], v155 offset:40960\n"
    "ds_read_b64_tr_b16 v[110:111], v155 offset:45056\n"
    "ds_read_b64_tr_b16 v[112:113], v154 offset:49152\n"
    "ds_read_b64_tr_b16 v[114:115], v154 offset:53248\n"
    "ds_read_b64_tr_b16 v[116:117], v155 offset:49152\n"
    "ds_read_b64_tr_b16 v[118:119], v155 offset:53248\n"
    "ds_read_b64_tr_b16 v[120:121], v154 offset:57344\n"
    "ds_read_b64_tr_b16 v[122:123], v154 offset:61440\n"
    "ds_read_b64_tr_b16 v[124:125], v155 offset:57344\n"
    "ds_read_b64_tr_b16 v[126:127], v155 offset:61440\n"
    "s_waitcnt lgkmcnt(14)\n"
    "v_mfma_f32_16x16x32_bf16 v[32:35], v[64:67], v[0:3], 0\n"
    "v_mfma_f32_16x16x32_bf16 v[36:39], v[68:71], v[0:3], 0\n"
    "v_mfma_f32_16x16x32_bf16 v[32:35], v[72:75], v[4:7], v[32:35]\n"
    "v_mfma_f32_16x16x32_bf16 v[36:39], v[76:79], v[4:7], v[36:39]\n"
    "v_mfma_f32_16x16x32_bf16 v[40:43], v[80:83], v[8:11], v[40:43]\n"
    "s_waitcnt lgkmcnt(12)\n"
    "v_mfma_f32_16x16x32_bf16 v[44:47], v[84:87], v[8:11], v[44:47]\n"
    "s_waitcnt lgkmcnt(10)\n"
    "v_mfma_f32_16x16x32_bf16 v[48:51], v[104:107], v[20:23], v[48:51]\n"
    "s_waitcnt lgkmcnt(8)\n"
    "v_mfma_f32_16x16x32_bf16 v[52:55], v[108:111], v[20:23], v[52:55]\n"
    "s_waitcnt lgkmcnt(6)\n"
    "v_mfma_f32_16x16x32_bf16 v[56:59], v[112:115], v[24:27], 0\n"
    "s_waitcnt lgkmcnt(4)\n"
    "v_mfma_f32_16x16x32_bf16 v[60:63], v[116:119], v[24:27], 0\n"
    "s_waitcnt lgkmcnt(2)\n"
    "v_mfma_f32_16x16x32_bf16 v[56:59], v[120:123], v[28:31], v[56:59]\n"
    "s_waitcnt lgkmcnt(0)\n"
    "v_mfma_f32_16x16x32_bf16 v[60:63], v[124:127], v[28:31], v[60:63]\n"
    "v_mul_f32 v32, v136, v32\n"
    "v_mul_f32 v33, v136, v33\n"
    "v_mul_f32 v34, v136, v34\n"
    "v_mul_f32 v35, v136, v35\n"
    "v_mul_f32 v36, v136, v36\n"
    "v_mul_f32 v37, v136, v37\n"
    "v_mul_f32 v38, v136, v38\n"
    "v_mul_f32 v39, v136, v39\n"
    "v_fmac_f32 v32, v138, v40\n"
    "v_fmac_f32 v33, v138, v41\n"
    "v_fmac_f32 v34, v138, v42\n"
    "v_fmac_f32 v35, v138, v43\n"
    "v_fmac_f32 v36, v138, v44\n"
    "v_fmac_f32 v37, v138, v45\n"
    "v_fmac_f32 v38, v138, v46\n"
    "v_fmac_f32 v39, v138, v47\n"
    "v_fmac_f32 v32, v140, v48\n"
    "v_fmac_f32 v33, v140, v49\n"
    "v_fmac_f32 v34, v140, v50\n"
    "v_fmac_f32 v35, v140, v51\n"
    "v_fmac_f32 v36, v140, v52\n"
    "v_fmac_f32 v37, v140, v53\n"
    "v_fmac_f32 v38, v140, v54\n"
    "v_fmac_f32 v39, v140, v55\n"
    "v_fmac_f32 v32, v142, v56\n"
    "v_fmac_f32 v33, v142, v57\n"
    "v_fmac_f32 v34, v142, v58\n"
    "v_fmac_f32 v35, v142, v59\n"
    "v_fmac_f32 v36, v142, v60\n"
    "v_fmac_f32 v37, v142, v61\n"
    "v_fmac_f32 v38, v142, v62\n"
    "v_fmac_f32 v39, v142, v63\n"
    "v_bfi_b32 v32, 1, v128, v32\n"
    "v_bfi_b32 v33, 1, v129, v33\n"
    "v_bfi_b32 v34, 1, v130, v34\n"
    "v_bfi_b32 v35, 1, v131, v35\n"
    "v_bfi_b32 v36, 1, v132, v36\n"
    "v_bfi_b32 v37, 1, v133, v37\n"
    "v_bfi_b32 v38, 1, v134, v38\n"
    "v_bfi_b32 v39, 1, v135, v39\n"
    "s_setprio 3\n"
    "s_waitcnt vmcnt(0)\n"
    "v_cmp_ne_u32 vcc, v156, v157\n"
    "s_cmp_eq_u32 s52, 0\n"
    "s_cbranch_scc1 L_pub%=\n"
    "s_cbranch_vccnz L_pub%=\n"
    "s_cmp_eq_u32 s31, 0\n"
    "s_cbranch_scc1 L_gend%=\n"
    "s_mov_b32 s54, 0\n"
    "L_chk%=:\n"
    "s_add_u32 s54, s54, 1\n"
    "s_cmp_gt_u32 s54, 65536\n"
    "s_cbranch_scc1 L_gend%=\n"
    "s_sleep 4\n"
    "buffer_load_dword v157, off, s[60:63], 0 sc1\n"
    "s_waitcnt vmcnt(0)\n"
    "v_cmp_ne_u32 vcc, v156, v157\n"
    "s_nop 1\n"
    "s_cbranch_vccz L_chk%=\n"
    "s_mov_b64 exec, s[58:59]\n"
    "s_cbranch_execz L_xw2%=\n"
    "buffer_store_dwordx4 v[32:35], v161, s[60:63], 0 offen offset:0 sc1\n"
    "buffer_store_dwordx4 v[36:39], v161, s[60:63], 0 offen offset:64 sc1\n"
    "s_cmp_eq_u32 s56, 0\n"
    "s_cbranch_scc0 L_xw2%=\n"
    "buffer_store_dwordx2 v[158:159], v162, s[60:63], 0 offen offset:512 sc1\n"
    "L_xw2%=:\n"
    "s_mov_b64 exec, -1\n"
    "s_branch L_gend%=\n"
    "L_pub%=:\n"
    "s_mov_b64 exec, s[58:59]\n"
    "s_cbranch_execz L_xw3%=\n"
    "buffer_store_dwordx4 v[32:35], v161, s[60:63], 0 offen offset:0 sc1\n"
    "buffer_store_dwordx4 v[36:39], v161, s[60:63], 0 offen offset:64 sc1\n"
    "s_cmp_eq_u32 s56, 0\n"
    "s_cbranch_scc0 L_xw3%=\n"
    "buffer_store_dwordx2 v[158:159], v162, s[60:63], 0 offen offset:512 sc1\n"
    "L_xw3%=:\n"
    "s_mov_b64 exec, -1\n"
    "s_cmp_eq_u32 s57, 0\n"
    "s_cbranch_scc0 L_gend%=\n"
    "s_cmp_eq_u32 s31, 0\n"
    "s_cbranch_scc1 L_gend%=\n"
    "v_mov_b32 v150, 0\n"
    "L_f5c_r%=:\n"
    "ds_read_b32 v151, v150 offset:3080\n"
    "L_f5c_w%=:\n"
    "s_waitcnt lgkmcnt(0)\n"
    "v_readfirstlane_b32 s13, v151\n"
    "s_nop 1\n"
    "s_cmp_ge_u32 s13, 4\n"
    "s_cbranch_scc1 L_f5c_d%=\n"
    "s_sleep 1\n"
    "ds_read_b32 v151, v150 offset:3080\n"
    "s_branch L_f5c_w%=\n"
    "L_f5c_d%=:\n"
    "v_lshrrev_b32 v24, 5, v166\n"
    "v_mul_u32_u24 v22, 5120, v24\n"
    "v_add_u32 v22, 128, v22\n"
    "v_and_b32 v24, 31, v166\n"
    "v_lshl_add_u32 v20, v24, 4, v22\n"
    "v_lshlrev_b32 v24, 3, v166\n"
    "s_lshl_b32 s50, s31, 8\n"
    "s_mov_b32 s55, 0\n"
    "s_mov_b32 s54, 0\n"
    "L_row%=:\n"
    "v_cmp_gt_u32_e64 s[70:71], s50, v24\n"
    "L_sweep%=:\n"
    "s_call_b64 s[68:69], L_gather%=\n"
    "v_xor_b32 v25, v25, v146\n"
    "v_xor_b32 v26, v26, v147\n"
    "v_or_b32 v26, 0xffff, v26\n"
    "v_and_b32 v25, v25, v26\n"
    "v_cmp_ne_u32 vcc, -1, v25\n"
    "s_add_u32 s54, s54, 1\n"
    "s_nop 0\n"
    "s_and_b64 vcc, vcc, s[70:71]\n"
    "s_cbranch_scc0 L_comb%=\n"
    "s_cmp_gt_u32 s54, 65536\n"
    "s_cbranch_scc1 L_comb%=\n"
    "s_and_b32 s13, s54, 3\n"
    "s_cbranch_scc1 L_sweep%=\n"
    "s_mov_b64 exec, 1\n"
    "buffer_atomic_add v160, off, s[60:63], 0\n"
    "s_mov_b64 exec, -1\n"
    "s_branch L_sweep%=\n"
    "L_comb%=:\n"
    "v_and_b32 v64, -2, v64\n"
    "v_and_b32 v65, -2, v65\n"
    "v_and_b32 v66, -2, v66\n"
    "v_and_b32 v67, -2, v67\n"
    "v_and_b32 v68, -2, v68\n"
    "v_and_b32 v69, -2, v69\n"
    "v_and_b32 v70, -2, v70\n"
    "v_and_b32 v71, -2, v71\n"
    "v_and_b32 v72, -2, v72\n"
    "v_and_b32 v73, -2, v73\n"
    "v_and_b32 v74, -2, v74\n"
    "v_and_b32 v75, -2, v75\n"
    "v_and_b32 v76, -2, v76\n"
    "v_and_b32 v77, -2, v77\n"
    "v_and_b32 v78, -2, v78\n"
    "v_and_b32 v79, -2, v79\n"
    "v_and_b32 v80, -2, v80\n"
    "v_and_b32 v81, -2, v81\n"
    "v_and_b32 v82, -2, v82\n"
    "v_and_b32 v83, -2, v83\n"
    "v_and_b32 v84, -2, v84\n"
    "v_and_b32 v85, -2, v85\n"
    "v_and_b32 v86, -2, v86\n"
    "v_and_b32 v87, -2, v87\n"
    "v_and_b32 v88, -2, v88\n"
    "v_and_b32 v89, -2, v89\n"
    "v_and_b32 v90, -2, v90\n"
    "v_and_b32 v91, -2, v91\n"
    "v_and_b32 v92, -2, v92\n"
    "v_and_b32 v93, -2, v93\n"
    "v_and_b32 v94, -2, v94\n"
    "v_and_b32 v95, -2, v95\n"
    "v_and_b32 v96, -2, v96\n"
    "v_and_b32 v97, -2, v97\n"
    "v_and_b32 v98, -2, v98\n"
    "v_and_b32 v99, -2, v99\n"
    "v_and_b32 v100, -2, v100\n"
    "v_and_b32 v101, -2, v101\n"
    "v_and_b32 v102, -2, v102\n"
    "v_and_b32 v103, -2, v103\n"
    "v_and_b32 v104, -2, v104\n"
    "v_and_b32 v105, -2, v105\n"
    "v_and_b32 v106, -2, v106\n"
    "v_and_b32 v107, -2, v107\n"
    "v_and_b32 v108, -2, v108\n"
    "v_and_b32 v109, -2, v109\n"
    "v_and_b32 v110, -2, v110\n"
    "v_and_b32 v111, -2, v111\n"
    "v_max3_f32 v8, v96, v98, v100\n"
    "v_max3_f32 v9, v102, v104, v106\n"
    "v_max3_f32 v8, v8, v108, v110\n"
    "v_max_f32 v8, v8, v9\n"
    "v_sub_f32 v96, v96, v8\n"
    "v_sub_f32 v98, v98, v8\n"
    "v_sub_f32 v100, v100, v8\n"
    "v_sub_f32 v102, v102, v8\n"
    "v_sub_f32 v104, v104, v8\n"
    "v_sub_f32 v106, v106, v8\n"
    "v_sub_f32 v108, v108, v8\n"
    "v_sub_f32 v110, v110, v8\n"
    "v_exp_f32 v96, v96\n"
    "v_exp_f32 v98, v98\n"
    "v_exp_f32 v100, v100\n"
    "v_exp_f32 v102, v102\n"
    "v_exp_f32 v104, v104\n"
    "v_exp_f32 v106, v106\n"
    "v_exp_f32 v108, v108\n"
    "v_exp_f32 v110, v110\n"
    "v_mul_f32 v9, v96, v97\n"
    "v_mul_f32 v12, v96, v64\n"
    "v_mul_f32 v13, v96, v65\n"
    "v_mul_f32 v14, v96, v66\n"
    "v_mul_f32 v15, v96, v67\n"
    "v_fmac_f32 v9, v98, v99\n"
    "v_fmac_f32 v12, v98, v68\n"
    "v_fmac_f32 v13, v98, v69\n"
    "v_fmac_f32 v14, v98, v70\n"
    "v_fmac_f32 v15, v98, v71\n"
    "v_fmac_f32 v9, v100, v101\n"
    "v_fmac_f32 v12, v100, v72\n"
    "v_fmac_f32 v13, v100, v73\n"
    "v_fmac_f32 v14, v100, v74\n"
    "v_fmac_f32 v15, v100, v75\n"
    "v_fmac_f32 v9, v102, v103\n"
    "v_fmac_f32 v12, v102, v76\n"
    "v_fmac_f32 v13, v102, v77\n"
    "v_fmac_f32 v14, v102, v78\n"
    "v_fmac_f32 v15, v102, v79\n"
    "v_fmac_f32 v9, v104, v105\n"
    "v_fmac_f32 v12, v104, v80\n"
    "v_fmac_f32 v13, v104, v81\n"
    "v_fmac_f32 v14, v104, v82\n"
    "v_fmac_f32 v15, v104, v83\n"
    "v_fmac_f32 v9, v106, v107\n"
    "v_fmac_f32 v12, v106, v84\n"
    "v_fmac_f32 v13, v106, v85\n"
    "v_fmac_f32 v14, v106, v86\n"
    "v_fmac_f32 v15, v106, v87\n"
    "v_fmac_f32 v9, v108, v109\n"
    "v_fmac_f32 v12, v108, v88\n"
    "v_fmac_f32 v13, v108, v89\n"
    "v_fmac_f32 v14, v108, v90\n"
    "v_fmac_f32 v15, v108, v91\n"
    "v_fmac_f32 v9, v110, v111\n"
    "v_fmac_f32 v12, v110, v92\n"
    "v_fmac_f32 v13, v110, v93\n"
    "v_fmac_f32 v14, v110, v94\n"
    "v_fmac_f32 v15, v110, v95\n"
    "v_rcp_f32 v9, v9\n"
    "s_nop 0\n"
    "v_mul_f32 v12, v9, v12\n"
    "v_mul_f32 v13, v9, v13\n"
    "v_mul_f32 v14, v9, v14\n"
    "v_mul_f32 v15, v9, v15\n"
    "v_cvt_pk_bf16_f32 v12, v12, v13\n"
    "v_cvt_pk_bf16_f32 v13, v14, v15\n"
    "buffer_store_dwordx2 v[12:13], v24, s[48:51], 0 offen nt\n"
    "v_add_u32 v20, 10240, v20\n"
    "v_add_u32 v22, 10240, v22\n"
    "v_mov_b32 v146, v148\n"
    "v_mov_b32 v147, v149\n"
    "v_add_u32 v24, 512, v24\n"
    "s_add_u32 s55, s55, 2\n"
    "s_cmp_lt_u32 s55, s31\n"
    "s_cbranch_scc1 L_row%=\n"
    "L_gend%=:\n"
    "s_endpgm\n"
    "L_gather%=:\n"
    "v_add_u32 v21, 2560, v20\n"
    "v_add_u32 v23, 2560, v22\n"
    "buffer_load_dwordx4 v[64:67], v20, s[60:63], 0 offen offset:0 sc1\n"
    "buffer_load_dwordx2 v[96:97], v22, s[60:63], 0 offen offset:512 sc1\n"
    "buffer_load_dwordx4 v[68:71], v20, s[60:63], 0 offen offset:640 sc1\n"
    "buffer_load_dwordx2 v[98:99], v22, s[60:63], 0 offen offset:1152 sc1\n"
    "buffer_load_dwordx4 v[72:75], v20, s[60:63], 0 offen offset:1280 sc1\n"
    "buffer_load_dwordx2 v[100:101], v22, s[60:63], 0 offen offset:1792 sc1\n"
    "buffer_load_dwordx4 v[76:79], v20, s[60:63], 0 offen offset:1920 sc1\n"
    "buffer_load_dwordx2 v[102:103], v22, s[60:63], 0 offen offset:2432 sc1\n"
    "buffer_load_dwordx4 v[80:83], v21, s[60:63], 0 offen offset:0 sc1\n"
    "buffer_load_dwordx2 v[104:105], v23, s[60:63], 0 offen offset:512 sc1\n"
    "buffer_load_dwordx4 v[84:87], v21, s[60:63], 0 offen offset:640 sc1\n"
    "buffer_load_dwordx2 v[106:107], v23, s[60:63], 0 offen offset:1152 sc1\n"
    "buffer_load_dwordx4 v[88:91], v21, s[60:63], 0 offen offset:1280 sc1\n"
    "buffer_load_dwordx2 v[108:109], v23, s[60:63], 0 offen offset:1792 sc1\n"
    "buffer_load_dwordx4 v[92:95], v21, s[60:63], 0 offen offset:1920 sc1\n"
    "buffer_load_dwordx2 v[110:111], v23, s[60:63], 0 offen offset:2432 sc1\n"
    "s_waitcnt vmcnt(0)\n"
    "L_pack%=:\n"
    "v_alignbit_b32 v25, v64, v25, 1\n"
    "v_alignbit_b32 v26, v96, v26, 1\n"
    "v_alignbit_b32 v25, v65, v25, 1\n"
    "v_alignbit_b32 v25, v66, v25, 1\n"
    "v_alignbit_b32 v26, v97, v26, 1\n"
    "v_alignbit_b32 v25, v67, v25, 1\n"
    "v_alignbit_b32 v25, v68, v25, 1\n"
    "v_alignbit_b32 v26, v98, v26, 1\n"
    "v_alignbit_b32 v25, v69, v25, 1\n"
    "v_alignbit_b32 v25, v70, v25, 1\n"
    "v_alignbit_b32 v26, v99, v26, 1\n"
    "v_alignbit_b32 v25, v71, v25, 1\n"
    "v_alignbit_b32 v25, v72, v25, 1\n"
    "v_alignbit_b32 v26, v100, v26, 1\n"
    "v_alignbit_b32 v25, v73, v25, 1\n"
    "v_alignbit_b32 v25, v74, v25, 1\n"
    "v_alignbit_b32 v26, v101, v26, 1\n"
    "v_alignbit_b32 v25, v75, v25, 1\n"
    "v_alignbit_b32 v25, v76, v25, 1\n"
    "v_alignbit_b32 v26, v102, v26, 1\n"
    "v_alignbit_b32 v25, v77, v25, 1\n"
    "v_alignbit_b32 v25, v78, v25, 1\n"
    "v_alignbit_b32 v26, v103, v26, 1\n"
    "v_alignbit_b32 v25, v79, v25, 1\n"
    "v_alignbit_b32 v25, v80, v25, 1\n"
    "v_alignbit_b32 v26, v104, v26, 1\n"
    "v_alignbit_b32 v25, v81, v25, 1\n"
    "v_alignbit_b32 v25, v82, v25, 1\n"
    "v_alignbit_b32 v26, v105, v26, 1\n"
    "v_alignbit_b32 v25, v83, v25, 1\n"
    "v_alignbit_b32 v25, v84, v25, 1\n"
    "v_alignbit_b32 v26, v106, v26, 1\n"
    "v_alignbit_b32 v25, v85, v25, 1\n"
    "v_alignbit_b32 v25, v86, v25, 1\n"
    "v_alignbit_b32 v26, v107, v26, 1\n"
    "v_alignbit_b32 v25, v87, v25, 1\n"
    "v_alignbit_b32 v25, v88, v25, 1\n"
    "v_alignbit_b32 v26, v108, v26, 1\n"
    "v_alignbit_b32 v25, v89, v25, 1\n"
    "v_alignbit_b32 v25, v90, v25, 1\n"
    "v_alignbit_b32 v26, v109, v26, 1\n"
    "v_alignbit_b32 v25, v91, v25, 1\n"
    "v_alignbit_b32 v25, v92, v25, 1\n"
    "v_alignbit_b32 v26, v110, v26, 1\n"
    "v_alignbit_b32 v25, v93, v25, 1\n"
    "v_alignbit_b32 v25, v94, v25, 1\n"
    "v_alignbit_b32 v26, v111, v26, 1\n"
    "v_alignbit_b32 v25, v95, v25, 1\n"
    "s_setpc_b64 s[68:69]\n"
    "L_fb%=:\n"
    "s_setprio 0\n"
    "s_waitcnt vmcnt(0) lgkmcnt(0)\n"
    :
    : [ka] "s"(__builtin_amdgcn_kernarg_segment_ptr()), [bid] "s"(bid), [wave] "s"(wave), [lds] "s"((unsigned)(size_t)(__attribute__((address_space(3))) char*)lds)
    : "v0", "v1", "v2", "v3", "v4", "v5", "v6", "v7", "v8", "v9", "v10", "v11", "v12", "v13", "v14", "v15", "v16", "v17", "v18", "v19", "v20", "v21", "v22",
      "v23", "v24", "v25", "v26", "v27", "v28", "v29", "v30", "v31", "v32", "v33", "v34", "v35", "v36", "v37", "v38", "v39", "v40", "v41", "v42", "v43",
      "v44", "v45", "v46", "v47", "v48", "v49", "v50", "v51", "v52", "v53", "v54", "v55", "v56", "v57", "v58", "v59", "v60", "v61", "v62", "v63", "v64",
      "v65", "v66", "v67", "v68", "v69", "v70", "v71", "v72", "v73", "v74", "v75", "v76", "v77", "v78", "v79", "v80", "v81", "v82", "v83", "v84", "v85",
      "v86", "v87", "v88", "v89", "v90", "v91", "v92", "v93", "v94", "v95", "v96", "v97", "v98", "v99", "v100", "v101", "v102", "v103", "v104", "v105",
      "v106", "v107", "v108", "v109", "v110", "v111", "v112", "v113", "v114", "v115", "v116", "v117", "v118", "v119", "v120", "v121", "v122", "v123",
      "v124", "v125", "v126", "v127", "v128", "v129", "v130", "v131", "v132", "v133", "v134", "v135", "v136", "v137", "v138", "v139", "v140", "v141",
      "v142", "v143", "v144", "v145", "v146", "v147", "v148", "v149", "v150", "v151", "v152", "v153", "v154", "v155", "v156", "v157", "v158", "v159",
      "v160", "v161", "v162", "v163", "v164", "v165", "v166", "v167", "s8", "s9", "s10", "s11", "s12", "s13", "s14", "s15", "s16", "s17", "s18", "s19",
      "s20", "s21", "s22", "s23", "s24", "s25", "s26", "s27", "s28", "s29", "s30", "s31", "s33", "s34", "s35", "s36", "s37", "s38", "s39", "s40", "s41",
      "s42", "s43", "s44", "s45", "s46", "s47", "s48", "s49", "s50", "s51", "s52", "s53", "s54", "s55", "s56", "s57", "s58", "s59", "s60", "s61", "s62",
      "s63", "s64", "s65", "s66", "s67", "s68", "s69", "s70", "s71", "s72", "s73", "s74", "s75", "s76", "s77", "s78", "s79", "s80", "s81", "s82", "s83",
      "s84", "s85", "s86", "s87", "s88", "s89", "s90", "s91", "s92", "s93", "s94", "s95", "s96", "s97", "s98", "s99", "vcc", "scc", "memory");
#endif
  const int lane = __builtin_amdgcn_mbcnt_hi(~0u, __builtin_amdgcn_mbcnt_lo(~0u, 0u));
  generic_rows(q, k, v, *pw, *png, out, bid * 12 + wave, 256 * 12, lane);
}

extern "C" void kernel_launch(void* const* d_in, const int* in_sizes, int n_in, void* d_out, int out_size, void* d_ws, size_t ws_size,
                              hipStream_t stream) {
  glw_attn_kernel<<<256, 768, 0, stream>>>((const unsigned*)d_in[0], (const unsigned*)d_in[1], (const unsigned*)d_in[2], (const int*)d_in[3],
                                           (const int*)d_in[4], (unsigned*)d_out, (char*)d_ws);
}
```
